```python
import torch

def _softmax(x, axis=-1):
    return torch.softmax(x, dim=axis)
def _gelu(x):
    return torch.nn.functional.gelu(x, approximate="tanh")
def _silu(x):
    return torch.nn.functional.silu(x)
def _sigmoid(x):
    return torch.sigmoid(x)
def _layernorm(x, axis=-1, eps=1e-5):
    m = x.mean(dim=axis, keepdim=True)
    v = x.var(dim=axis, keepdim=True, unbiased=False)
    return (x - m) / torch.sqrt(v + eps)
def _scatter_add(tgt, idx, src, axis=0):
    return tgt.clone().index_add_(axis, idx, src)
def _segment_sum(src, seg, n):
    out = torch.zeros((n,) + src.shape[1:], dtype=src.dtype)
    return out.index_add_(0, seg, src)
def _pad(x, pw):
    flat = tuple(v for pair in reversed(pw) for v in pair)
    return torch.nn.functional.pad(x, flat)

kernel_name = 'parallel_sum__softmax_full_mha__geglu_ffn__prompt'

def setup_inputs():

    _w = (lambda a: (lambda t: t.to(torch.bfloat16) if t.is_floating_point() else t)(torch.as_tensor(a)))
    def _A_setup():
        B, S, D = 1, 2048, 640
        return {
            "x": _w((lambda *s: torch.randn(*s, dtype=torch.bfloat16) * 0.05)(B, S, D)),
            "wqkv": _w((lambda *s: torch.randn(*s, dtype=torch.bfloat16) * 0.2)(D, 3 * D)),
            "wo": _w((lambda *s: torch.randn(*s, dtype=torch.bfloat16) * 0.1)(D, D)),
        }
    _A_in = _A_setup()

    def _B_setup():
        B, S, D, F = 1, 2048, 640, 1728
        return {
            "x": _w((lambda *s: torch.randn(*s, dtype=torch.bfloat16) * 0.05)(B, S, D)),
            "w_gate": _w((lambda *s: torch.randn(*s, dtype=torch.bfloat16) * 0.1)(D, F)),
            "w_up": _w((lambda *s: torch.randn(*s, dtype=torch.bfloat16) * 0.1)(D, F)),
            "w_down": _w((lambda *s: torch.randn(*s, dtype=torch.bfloat16) * 0.1)(F, D)),
        }
    _B_in = _B_setup()

    return {
        "A_x": _A_in["x"],
        "A_wqkv": _A_in["wqkv"],
        "A_wo": _A_in["wo"],
        "B_x": _B_in["x"],
        "B_w_gate": _B_in["w_gate"],
        "B_w_up": _B_in["w_up"],
        "B_w_down": _B_in["w_down"],
    }

def reference(A_x, A_wqkv, A_wo, B_x, B_w_gate, B_w_up, B_w_down):
    def _A_ref(x, wqkv, wo):
        B, S, D = x.shape
        H = 32; Dh = D // H
        qkv = (lambda x, *d: x.permute(*d))((x @ wqkv).reshape(B, S, 3, H, Dh), 2, 0, 3, 1, 4)
        q, k, v = qkv[0], qkv[1], qkv[2]
        scale = Dh ** -0.5
        scores = torch.einsum("bhqd,bhkd->bhqk", q, k) * scale
        p = _softmax(scores, axis=-1)
        o = torch.einsum("bhqk,bhkd->bhqd", p, v)
        return (lambda x, *d: x.permute(*d))(o, 0, 2, 1, 3).reshape(B, S, D) @ wo

    def _B_ref(x, w_gate, w_up, w_down):
        return (_gelu(x @ w_gate) * (x @ w_up)) @ w_down

    x = A_x
    xn = _layernorm(x, axis=-1)
    return x + _A_ref(xn, A_wqkv, A_wo) + _B_ref(xn, B_w_gate, B_w_up, B_w_down)
```

```cpp
#include <hip/hip_runtime.h>
#include <hip/hip_bf16.h>
#include <stdint.h>

typedef __attribute__((ext_vector_type(8))) short bf16x8;
typedef __attribute__((ext_vector_type(4))) short s16x4;
typedef __attribute__((ext_vector_type(2))) __bf16 bf16x2;
typedef __attribute__((ext_vector_type(16))) float f32x16;
typedef __attribute__((ext_vector_type(4))) float f32x4;
typedef __attribute__((ext_vector_type(4))) unsigned u32x4;
typedef __attribute__((ext_vector_type(2))) unsigned u32x2;
typedef __attribute__((address_space(3))) const char* lds_cptr;
typedef __attribute__((address_space(3))) char* lds_ptr;

constexpr int SEQ = 2048, DM = 640, NH = 32, DH = 20, FF = 1728;
constexpr int NUNIT = 148;
constexpr size_t WS_Q = 0;
constexpr size_t WS_KV = WS_Q + (size_t)NH * 64 * 1280;
constexpr size_t WS_XN = WS_KV + (size_t)NH * 64 * 2560;
constexpr size_t WS_AF = WS_XN + (size_t)64 * 40 * 1024;

#define SBAR() __builtin_amdgcn_sched_barrier(0)
#define PIN(x) asm volatile("" : "+v"(x))
#define MFMA32(a, b, c) __builtin_amdgcn_mfma_f32_32x32x16_bf16(a, b, c, 0, 0, 0)
#define MFMA32K8(a, b, c) __builtin_amdgcn_mfma_f32_32x32x8bf16_1k(a, b, c, 0, 0, 0)
#define MFMA16(a, b, c) __builtin_amdgcn_mfma_f32_16x16x32_bf16(a, b, c, 0, 0, 0)
#define WAIT_BAR(N) asm volatile("s_waitcnt vmcnt(" #N ") lgkmcnt(0)\n\ts_barrier" ::: "memory")
#define WAIT_VM(N) asm volatile("s_waitcnt vmcnt(" #N ")" ::: "memory")

__device__ __forceinline__ unsigned cvtpk(float lo, float hi) {
    unsigned r; asm("v_cvt_pk_bf16_f32 %0, %1, %2" : "=v"(r) : "v"(lo), "v"(hi)); return r; }
__device__ __forceinline__ void glds16(const void* sbase, unsigned voff, unsigned lds_base) {
    unsigned sv;
    asm volatile("s_mov_b32 %0, m0\n\ts_mov_b32 m0, %3\n\ts_nop 0\n\tglobal_load_lds_dwordx4 %1, %2\n\ts_mov_b32 m0, %0"
                 : "=&s"(sv) : "v"(voff), "s"(sbase), "s"(lds_base) : "memory"); }
__device__ __forceinline__ bf16x8 ldsr128(lds_cptr p) { return *(const __attribute__((address_space(3))) bf16x8*)p; }
__device__ __forceinline__ s16x4 ldsr64(lds_cptr p) { return *(const __attribute__((address_space(3))) s16x4*)p; }
__device__ __forceinline__ s16x4 ldstr(lds_cptr p) {
    return __builtin_amdgcn_ds_read_tr16_b64_v4i16((__attribute__((address_space(3))) s16x4*)p); }
__device__ __forceinline__ bf16x8 cat(s16x4 a, s16x4 b) { return __builtin_shufflevector(a, b, 0, 1, 2, 3, 4, 5, 6, 7); }
__device__ __forceinline__ float dot2(unsigned a, unsigned b, float c) {
    return __builtin_amdgcn_fdot2_f32_bf16(__builtin_bit_cast(bf16x2, a), __builtin_bit_cast(bf16x2, b), c, false); }
__device__ __forceinline__ float xhalf(float v) {
    return __shfl_xor(v, 32); }
constexpr unsigned ONES2 = 0x3f803f80u;
constexpr float LN_EPS = 1e-5f;

constexpr int K1_STAGE = 32768, K1_LDS = 4 * K1_STAGE + 3 * 512;

template <bool VT>
__device__ __forceinline__ void qkv_block(const __hip_bfloat16* __restrict__ x, const __hip_bfloat16* __restrict__ w,
                                          char* __restrict__ ws, char* lds, int rb, int cb) {
    const int tid = threadIdx.x, l = tid & 63, wv = __builtin_amdgcn_readfirstlane(tid >> 6);
    const int wm = wv >> 1, wn = wv & 1, h = l >> 5, r32 = l & 31;
    const unsigned lds0 = (unsigned)(uintptr_t)lds;
    unsigned xo[4], wo_[4];
#pragma unroll
    for (int i = 0; i < 4; ++i) {
        const int r = 32 * wv + 8 * i + (l >> 3);
        xo[i] = (unsigned)(r * DM + (((l & 7) ^ ((r >> 1) & 7)) * 8)) * 2;
        const int kr = 16 * wv + 4 * i + (l >> 4);
        wo_[i] = (unsigned)(kr * (3 * DM) + (((l & 15) ^ ((kr & 3) << 2)) * 8)) * 2;
    }
    const char* xb = (const char*)(x + (size_t)rb * 128 * DM);
    const char* wb = (const char*)(w + cb * 128);
    auto stage = [&](int kt) {
        const unsigned d = lds0 + (kt & 3) * K1_STAGE + wv * 4096;
#pragma unroll
        for (int i = 0; i < 4; ++i) glds16(xb + kt * 128, xo[i], d + i * 1024);
#pragma unroll
        for (int i = 0; i < 4; ++i) glds16(wb + (size_t)kt * 64 * 3 * DM * 2, wo_[i], d + 16384 + i * 1024);
    };
    stage(0); stage(1); stage(2);
    int xa[4], wa[2];
#pragma unroll
    for (int ks = 0; ks < 4; ++ks) xa[ks] = (wm * 64 + r32) * 128 + (((2 * ks + h) ^ ((r32 >> 1) & 7)) * 16);
    const int xt0 = wn * 4096, xt1 = 4096 - xt0;
    {
        const int gi = l >> 4, p = l & 3, q = (l >> 2) & 3;
#pragma unroll
        for (int nt = 0; nt < 2; ++nt) {
            const int ch = wn * 8 + (nt ^ wm) * 4 + (gi & 1) * 2 + (p >> 1);
            wa[nt] = 16384 + ((gi >> 1) * 8 + q) * 256 + ((ch ^ (q << 2)) * 16) + (p & 1) * 8;
        }
    }
    f32x16 acc[2][2] = {};
    float s1 = 0.f, s2 = 0.f, cs = 0.f;
#pragma unroll
    for (int kt = 0; kt < 10; ++kt) {
        if (kt < 8) { WAIT_BAR(16); } else if (kt == 8) { WAIT_BAR(8); } else { WAIT_BAR(0); }
        if (kt + 3 < 10) stage(kt + 3);
        const lds_cptr sb = (lds_cptr)lds + (kt & 3) * K1_STAGE;
#pragma unroll
        for (int ks = 0; ks < 4; ++ks) {
            bf16x8 xf[2], wf[2];
#pragma unroll
            for (int t = 0; t < 2; ++t) {
                xf[t] = ldsr128(sb + xa[ks] + (t ? xt1 : xt0));
                wf[t] = cat(ldstr(sb + wa[t] + ks * 4096), ldstr(sb + wa[t] + ks * 4096 + 1024));
            }
#pragma unroll
            for (int mt = 0; mt < 2; ++mt)
#pragma unroll
                for (int nt = 0; nt < 2; ++nt)
                    acc[mt][nt] = VT ? MFMA32(xf[mt], wf[nt], acc[mt][nt]) : MFMA32(wf[nt], xf[mt], acc[mt][nt]);
            const u32x4 xs = __builtin_bit_cast(u32x4, xf[0]);
            const u32x4 wsum = __builtin_bit_cast(u32x4, wf[0]);
#pragma unroll
            for (int j = 0; j < 4; ++j) { s1 = dot2(xs[j], ONES2, s1); s2 = dot2(xs[j], xs[j], s2); cs = dot2(wsum[j], ONES2, cs); }
        }
    }
    s1 += xhalf(s1); s2 += xhalf(s2); cs += xhalf(cs);
    float* st = (float*)(lds + 4 * K1_STAGE);
    if (h == 0) {
        const float mu = s1 * (1.f / DM), var = fmaxf(s2 * (1.f / DM) - mu * mu, 0.f);
        st[wm * 64 + wn * 32 + r32] = mu;
        st[128 + wm * 64 + wn * 32 + r32] = rsqrtf(var + LN_EPS);
        st[256 + wn * 64 + wm * 32 + r32] = cs;
    }
    __syncthreads();
    if (!VT) {
        const bool isq = cb < 5;
        const float osc = isq ? 0.22360679774997896f * 1.4426950408889634f : 1.f;
        char* ob = ws + (isq ? WS_Q : WS_KV);
        const int tstr = isq ? 1280 : 2560, f00 = cb * 128 - (isq ? 0 : DM);
#pragma unroll
        for (int mt = 0; mt < 2; ++mt) {
            const int am = mt ^ wn;
            const float mu = st[wm * 64 + am * 32 + r32], rs = st[128 + wm * 64 + am * 32 + r32] * osc;
            const int tile = rb * 4 + wm * 2 + am;
#pragma unroll
            for (int nt = 0; nt < 2; ++nt)
#pragma unroll
                for (int rg = 0; rg < 4; ++rg) {
                    const int fl = wn * 64 + (nt ^ wm) * 32 + 8 * rg + 4 * h, f = f00 + fl, head = f / 20, dq = (f % 20) >> 2;
                    const f32x4 c4 = *(const f32x4*)(st + 256 + fl);
                    float v[4];
#pragma unroll
                    for (int j = 0; j < 4; ++j) v[j] = (acc[mt][nt][rg * 4 + j] - mu * c4[j]) * rs;
                    const int o = dq < 4 ? ((dq >> 1) * 32 + r32) * 16 + (dq & 1) * 8 : 1024 + r32 * 8;
                    u32x2 pk = {cvtpk(v[0], v[1]), cvtpk(v[2], v[3])};
                    *(u32x2*)(ob + (size_t)(head * 64 + tile) * tstr + o) = pk;
                }
        }
    } else {
#pragma unroll
        for (int mt = 0; mt < 2; ++mt) {
            const int am = mt ^ wn, tile = rb * 4 + wm * 2 + am;
            f32x4 mu4[4], rs4[4];
#pragma unroll
            for (int rg = 0; rg < 4; ++rg) {
                mu4[rg] = *(const f32x4*)(st + wm * 64 + am * 32 + 8 * rg + 4 * h);
                rs4[rg] = *(const f32x4*)(st + 128 + wm * 64 + am * 32 + 8 * rg + 4 * h);
            }
#pragma unroll
            for (int nt = 0; nt < 2; ++nt) {
                const int fl = wn * 64 + (nt ^ wm) * 32 + r32, f = (cb - 10) * 128 + fl, head = f / 20, d = f % 20;
                const float c = st[256 + fl];
#pragma unroll
                for (int s = 0; s < 2; ++s) {
                    float v[8];
#pragma unroll
                    for (int j = 0; j < 8; ++j) v[j] = (acc[mt][nt][8 * s + j] - mu4[2 * s + (j >> 2)][j & 3] * c) * rs4[2 * s + (j >> 2)][j & 3];
                    u32x4 pk = {cvtpk(v[0], v[1]), cvtpk(v[2], v[3]), cvtpk(v[4], v[5]), cvtpk(v[6], v[7])};
                    *(u32x4*)(ws + WS_KV + (size_t)(head * 64 + tile) * 2560 + 1280 + ((s * 2 + h) * 20 + d) * 16) = pk;
                }
            }
        }
    }
}

__device__ __forceinline__ void ln_block(const __hip_bfloat16* __restrict__ x, char* __restrict__ ws, int rb) {
    const int tid = threadIdx.x, l = tid & 63, wv = tid >> 6, h = l >> 5, r32 = l & 31;
    const int rt = rb * 4 + wv;
    const u32x4* src = (const u32x4*)(x + (size_t)(rt * 32 + r32) * DM + 8 * h);
    u32x4 v[40];
#pragma unroll
    for (int i = 0; i < 40; ++i) v[i] = src[2 * i];
    float s1 = 0.f, s2 = 0.f;
#pragma unroll
    for (int i = 0; i < 40; ++i)
#pragma unroll
        for (int j = 0; j < 4; ++j) { s1 = dot2(v[i][j], ONES2, s1); s2 = dot2(v[i][j], v[i][j], s2); }
    s1 += xhalf(s1); s2 += xhalf(s2);
    const float mu = s1 * (1.f / DM), rs = rsqrtf(fmaxf(s2 * (1.f / DM) - mu * mu, 0.f) + LN_EPS), nb = -mu * rs;
    u32x4* dst = (u32x4*)(ws + WS_XN + (size_t)rt * 40 * 1024) + l;
#pragma unroll
    for (int i = 0; i < 40; ++i) {
        u32x4 o;
#pragma unroll
        for (int j = 0; j < 4; ++j) {
            const float lo = __uint_as_float(v[i][j] << 16), hi = __uint_as_float(v[i][j] & 0xffff0000u);
            o[j] = cvtpk(fmaf(lo, rs, nb), fmaf(hi, rs, nb));
        }
        dst[i * 64] = o;
    }
}

__global__ __launch_bounds__(256, 1) void k1_qkv(const __hip_bfloat16* __restrict__ x, const __hip_bfloat16* __restrict__ wqkv, char* __restrict__ ws) {
    __shared__ __attribute__((aligned(16))) char lds[K1_LDS];
    const int b = blockIdx.x, xcd = b & 7, i = b >> 3;
    if (i < 30) {
        const int li = (xcd & 1) * 30 + i, rb = (xcd >> 1) * 4 + (li & 3), cb = li >> 2;
        if (cb >= 10) qkv_block<true>(x, wqkv, ws, lds, rb, cb); else qkv_block<false>(x, wqkv, ws, lds, rb, cb);
    } else {
        ln_block(x, ws, xcd * 2 + (i - 30));
    }
}

constexpr int AT_PAIR = 5136, AT_CHUNK = 4 * AT_PAIR;
constexpr int GU_STAGE = 14336 + 8192;
constexpr int K2_LDS = 3 * GU_STAGE;
constexpr float SHIFT_MARGIN = 64.f;

#define EXP8(P, S, o) do { \
    float e0 = __builtin_amdgcn_exp2f(S[o + 0]), e1 = __builtin_amdgcn_exp2f(S[o + 1]); \
    float e2 = __builtin_amdgcn_exp2f(S[o + 2]), e3 = __builtin_amdgcn_exp2f(S[o + 3]); \
    P[((o) >> 1) & 3] = cvtpk(e0, e1); P[(((o) >> 1) & 3) + 1] = cvtpk(e2, e3); } while (0)

__device__ __forceinline__ float max16(const f32x16& s) {
    float a = fmaxf(fmaxf(s[0], s[1]), s[2]), b = fmaxf(fmaxf(s[3], s[4]), s[5]);
    a = fmaxf(fmaxf(a, s[6]), s[7]); b = fmaxf(fmaxf(b, s[8]), s[9]);
    a = fmaxf(fmaxf(a, s[10]), s[11]); b = fmaxf(fmaxf(b, s[12]), s[13]);
    return fmaxf(fmaxf(a, s[14]), fmaxf(b, s[15]));
}
__device__ __forceinline__ f32x16 splat16(float v) { f32x16 r;
#pragma unroll
    for (int i = 0; i < 16; ++i) r[i] = v;
    return r; }

__device__ __forceinline__ f32x16 attn_fallback(const char* __restrict__ kv, bf16x8 q1, s16x4 q2, int l) {
    const int h = l >> 5, r32 = l & 31;
    float m = -INFINITY; f32x16 o = {};
    const f32x16 z = {};
    for (int t = 0; t < 64; ++t) {
        const char* p = kv + t * 2560;
        const bf16x8 k1 = *(const bf16x8*)(p + l * 16); const s16x4 k2 = *(const s16x4*)(p + 1024 + r32 * 8);
        f32x16 s = MFMA32(k1, q1, z); s = MFMA32K8(k2, q2, s);
        float tm = max16(s); tm = fmaxf(tm, xhalf(tm));
        const float mn = fmaxf(m, tm), al = __builtin_amdgcn_exp2f(m - mn); m = mn;
        u32x4 p0, p1;
#pragma unroll
        for (int i = 0; i < 16; ++i) { s[i] -= mn; o[i] *= al; }
        EXP8(p0, s, 0); EXP8(p0, s, 4); EXP8(p1, s, 8); EXP8(p1, s, 12);
        const u32x4 ones = {ONES2, ONES2, ONES2, ONES2};
        const int d = r32 < 20 ? r32 : 0;
        u32x4 v0 = *(const u32x4*)(p + 1280 + (h * 20 + d) * 16), v1 = *(const u32x4*)(p + 1280 + ((2 + h) * 20 + d) * 16);
        if (r32 >= 20) { v0 = ones; v1 = ones; }
        o = MFMA32(__builtin_bit_cast(bf16x8, v0), __builtin_bit_cast(bf16x8, p0), o);
        o = MFMA32(__builtin_bit_cast(bf16x8, v1), __builtin_bit_cast(bf16x8, p1), o);
    }
    return o;
}

__device__ __forceinline__ void attn_finish(f32x16 o, const char* __restrict__ kv, bf16x8 q1, s16x4 q2, char* __restrict__ ws, int head, int rt, int l) {
    const int h = l >> 5, r32 = l & 31;
    float big = fmaxf(fmaxf(fabsf(o[0]), fabsf(o[1])), fmaxf(fabsf(o[2]), fabsf(o[3])));
    bool ok = true;
#pragma unroll
    for (int i = 0; i < 9; ++i) ok = ok && (fabsf(o[i]) < 1e37f);
    if (h == 0) {
#pragma unroll
        for (int i = 9; i < 12; ++i) ok = ok && (fabsf(o[i]) < 1e37f);
    }
    (void)big;
    if (__builtin_expect(__any(!ok), 0)) o = attn_fallback(kv, q1, q2, l);
    const float lx = xhalf(o[8]), inv = 1.f / (h ? o[8] : lx);
    char* dst = ws + WS_AF + (size_t)rt * NUNIT * 1024;
    u32x4 a = {cvtpk(o[0] * inv, o[1] * inv), cvtpk(o[2] * inv, o[3] * inv), cvtpk(o[4] * inv, o[5] * inv), cvtpk(o[6] * inv, o[7] * inv)};
    *(u32x4*)(dst + head * 1024 + l * 16) = a;
    if (h == 0) {
        u32x2 e = {cvtpk(o[8] * inv, o[9] * inv), cvtpk(o[10] * inv, o[11] * inv)};
        *(u32x2*)(dst + (32 + (head >> 2)) * 1024 + ((head & 1) * 32 + r32) * 16 + ((head >> 1) & 1) * 8) = e;
    }
}

__device__ __forceinline__ void attn_block(char* __restrict__ ws, char* lds, int head, int qb) {
    const int tid = threadIdx.x, l = tid & 63, wv = __builtin_amdgcn_readfirstlane(tid >> 6), h = l >> 5, r32 = l & 31;
    const unsigned lds0 = (unsigned)(uintptr_t)lds;
    const char* kv = ws + WS_KV + (size_t)head * 64 * 2560;
    const unsigned voff = l * 16;
    auto stage = [&](int c, int slot) {
        const char* s = kv + (c * 4 + wv) * 5120;
        const unsigned d = lds0 + slot * AT_CHUNK + wv * AT_PAIR;
#pragma unroll
        for (int i = 0; i < 5; ++i) glds16(s + i * 1024, voff, d + i * 1024);
    };
    stage(0, 0); stage(1, 1);
    if (tid < 48) *(__attribute__((address_space(3))) unsigned*)((lds_ptr)lds + (tid >> 2) * AT_PAIR + 5120 + (tid & 3) * 4) = ONES2;
    const int rt0 = qb * 8 + wv * 2;
    const char* qp = ws + WS_Q + (size_t)(head * 64 + rt0) * 1280;
    const bf16x8 qa1 = *(const bf16x8*)(qp + l * 16), qb1 = *(const bf16x8*)(qp + 1280 + l * 16);
    s16x4 qa2 = *(const s16x4*)(qp + 1024 + r32 * 8), qb2 = *(const s16x4*)(qp + 1280 + 1024 + r32 * 8);
    if (h) { qa2 = s16x4{0, 0, 0, 0}; qb2 = s16x4{0, 0, 0, 0}; }
    const int ka1 = l * 16, ka2 = 1024 + r32 * 8;
    int va[2][2];
#pragma unroll
    for (int tp = 0; tp < 2; ++tp)
#pragma unroll
        for (int s = 0; s < 2; ++s) {
            const int d = r32 >= 28 ? r32 - 24 : r32;
            va[tp][s] = d < 20 ? tp * 2560 + 1280 + ((s * 2 + h) * 20 + d) * 16 : 5120;
        }
    WAIT_BAR(5);
    f32x16 nma, nmb;
    {
        const f32x16 z = {};
        float ma = -INFINITY, mb = -INFINITY;
#pragma unroll
        for (int t = 0; t < 4; ++t) {
            const lds_cptr p = (lds_cptr)lds + (t >> 1) * AT_PAIR + (t & 1) * 2560;
            const bf16x8 k1 = ldsr128(p + ka1); const s16x4 k2 = ldsr64(p + ka2);
            f32x16 sa = MFMA32(k1, qa1, z), sb = MFMA32(k1, qb1, z);
            sa = MFMA32K8(k2, qa2, sa); sb = MFMA32K8(k2, qb2, sb);
            ma = fmaxf(ma, max16(sa)); mb = fmaxf(mb, max16(sb));
        }
        ma = fmaxf(ma, xhalf(ma)); mb = fmaxf(mb, xhalf(mb));
        nma = splat16(-(ma + SHIFT_MARGIN)); nmb = splat16(-(mb + SHIFT_MARGIN));
        PIN(nma); PIN(nmb);
    }
    f32x16 oa = {}, ob = {}, sa, sb;
    u32x4 pa0, pa1, pb0 = {}, pb1 = {};
    bf16x8 k1, v0 = {}, v1 = {}; s16x4 k2;
    k1 = ldsr128((lds_cptr)lds + ka1); k2 = ldsr64((lds_cptr)lds + ka2);
    sa = MFMA32(k1, qa1, nma); sa = MFMA32K8(k2, qa2, sa);
    int slot = 0;
    for (int c = 0; c < 8; ++c) {
        const lds_cptr cb = (lds_cptr)lds + slot * AT_CHUNK;
        const int nslot = slot == 2 ? 0 : slot + 1;
        const lds_cptr nb = (lds_cptr)lds + nslot * AT_CHUNK;
#pragma unroll
        for (int t = 0; t < 8; ++t) {
            if (t == 7) {
                if (c < 6) { WAIT_BAR(0); stage(c + 2, slot == 0 ? 2 : slot - 1); } else if (c == 6) { WAIT_BAR(0); }
            }
            const lds_cptr kp = t < 7 ? cb + ((t + 1) >> 1) * AT_PAIR + ((t + 1) & 1) * 2560 : nb;
            const lds_cptr vp = cb + (t >> 1) * AT_PAIR;
            const bf16x8 nk1 = ldsr128(kp + ka1); const s16x4 nk2 = ldsr64(kp + ka2);
            const bf16x8 nv0 = ldsr128(vp + va[t & 1][0]), nv1 = ldsr128(vp + va[t & 1][1]);
            SBAR();
            sb = MFMA32(k1, qb1, nmb); SBAR();
            EXP8(pa0, sa, 0); PIN(pa0); SBAR();
            sb = MFMA32K8(k2, qb2, sb); SBAR();
            EXP8(pa0, sa, 4); PIN(pa0); SBAR();
            ob = MFMA32(v0, __builtin_bit_cast(bf16x8, pb0), ob); SBAR();
            EXP8(pa1, sa, 8); PIN(pa1); SBAR();
            ob = MFMA32(v1, __builtin_bit_cast(bf16x8, pb1), ob); SBAR();
            EXP8(pa1, sa, 12); PIN(pa1); SBAR();
            k1 = nk1; k2 = nk2; v0 = nv0; v1 = nv1;
            sa = MFMA32(k1, qa1, nma); SBAR();
            EXP8(pb0, sb, 0); PIN(pb0); SBAR();
            sa = MFMA32K8(k2, qa2, sa); SBAR();
            EXP8(pb0, sb, 4); PIN(pb0); SBAR();
            oa = MFMA32(v0, __builtin_bit_cast(bf16x8, pa0), oa); SBAR();
            EXP8(pb1, sb, 8); PIN(pb1); SBAR();
            oa = MFMA32(v1, __builtin_bit_cast(bf16x8, pa1), oa); SBAR();
            EXP8(pb1, sb, 12); PIN(pb1); SBAR();
        }
        slot = nslot;
    }
    ob = MFMA32(v0, __builtin_bit_cast(bf16x8, pb0), ob);
    ob = MFMA32(v1, __builtin_bit_cast(bf16x8, pb1), ob);
    attn_finish(oa, kv, qa1, qa2, ws, head, rt0, l);
    attn_finish(ob, kv, qb1, qb2, ws, head, rt0 + 1, l);
}

__device__ __forceinline__ void gu_block(const __hip_bfloat16* __restrict__ wg, const __hip_bfloat16* __restrict__ wu,
                                         char* __restrict__ ws, char* lds, int rb, int cbk) {
    const int tid = threadIdx.x, l = tid & 63, wv = __builtin_amdgcn_readfirstlane(tid >> 6);
    const unsigned lds0 = (unsigned)(uintptr_t)lds;
    const int u0 = (cbk * 27) >> 2, rt = rb * 4 + wv;
    const char* wlo = (const char*)(wg < wu ? wg : wu);
    const size_t dg = (const char*)wg - wlo, du = (const char*)wu - wlo;
    const char* wsrc[4];
#pragma unroll
    for (int i = 0; i < 4; ++i) {
        const int idx = wv * 224 + i * 64 + (i == 3 ? (l & 31) : l), row = idx / 28, ch = idx % 28;
        wsrc[i] = wlo + (ch < 14 ? dg : du) + (size_t)row * FF * 2 + u0 * 32 + (ch < 14 ? ch : ch - 14) * 16;
    }
    const char* xsrc = ws + WS_XN + (size_t)rt * 40 * 1024;
    auto stage = [&](int kt, int slot) {
        const unsigned d = lds0 + slot * GU_STAGE;
#pragma unroll
        for (int i = 0; i < 4; ++i) {
            const char* g = wsrc[i] + (size_t)kt * 32 * FF * 2;
            const unsigned dd = d + wv * 3584 + i * 1024;
            unsigned sv;
            if (i < 3 || l < 32)
                asm volatile("s_mov_b32 %0, m0\n\ts_mov_b32 m0, %2\n\ts_nop 0\n\tglobal_load_lds_dwordx4 %1, off\n\ts_mov_b32 m0, %0"
                             : "=&s"(sv) : "v"(g), "s"(dd) : "memory");
        }
        glds16(xsrc + kt * 2048, l * 16, d + 14336 + wv * 2048);
        glds16(xsrc + kt * 2048 + 1024, l * 16, d + 14336 + wv * 2048 + 1024);
    };
    stage(0, 0); stage(1, 1);
    const int gi = l >> 4;
    const int wa = ((gi >> 1) * 8 + ((l >> 2) & 3)) * 448 + (gi & 1) * 224 + (l & 3) * 8;
    const int xa = 14336 + wv * 2048 + l * 16;
    f32x16 acc[7] = {};
    int slot = 0;
    for (int kt = 0; kt < 20; ++kt) {
        if (kt < 19) { WAIT_BAR(6); } else { WAIT_BAR(0); }
        if (kt < 18) stage(kt + 2, slot == 0 ? 2 : slot - 1);
        const lds_cptr sb = (lds_cptr)lds + slot * GU_STAGE;
#pragma unroll
        for (int ks = 0; ks < 2; ++ks) {
            const bf16x8 xf = ldsr128(sb + xa + ks * 1024);
#pragma unroll
            for (int u = 0; u < 7; ++u) {
                const bf16x8 wf = cat(ldstr(sb + wa + ks * 16 * 448 + u * 32), ldstr(sb + wa + ks * 16 * 448 + 4 * 448 + u * 32));
                acc[u] = MFMA32(wf, xf, acc[u]);
            }
        }
        slot = slot == 2 ? 0 : slot + 1;
    }
    char* dst = ws + WS_AF + ((size_t)rt * NUNIT + 40 + u0) * 1024 + l * 16;
#pragma unroll
    for (int u = 0; u < 7; ++u) {
        float hv[8];
#pragma unroll
        for (int j = 0; j < 8; ++j) {
            const float g = acc[u][j];
            const float e = __builtin_amdgcn_exp2f(g * fmaf(g * g, -0.1029432397f, -2.302208198f));
            hv[j] = g * __builtin_amdgcn_rcpf(1.f + e) * acc[u][8 + j];
        }
        u32x4 pk = {cvtpk(hv[0], hv[1]), cvtpk(hv[2], hv[3]), cvtpk(hv[4], hv[5]), cvtpk(hv[6], hv[7])};
        *(u32x4*)(dst + u * 1024) = pk;
    }
}

__global__ __launch_bounds__(256, 2) void k2_attn_gu(const __hip_bfloat16* __restrict__ wg, const __hip_bfloat16* __restrict__ wu, char* __restrict__ ws) {
    __shared__ __attribute__((aligned(16))) char lds[K2_LDS];
    const int b = blockIdx.x & 255, xcd = b & 7, i = b >> 3;
    if (blockIdx.x < 256) attn_block(ws, lds, xcd * 4 + (i & 3), i >> 2);
    else gu_block(wg, wu, ws, lds, (xcd >> 2) * 8 + (i & 7), (xcd & 3) * 4 + (i >> 3));
}

constexpr int K3_STEP = 9216, K3_RING = 4, K3_LDS = 4 * K3_RING * K3_STEP;

__global__ __launch_bounds__(256, 1) void k3_out(const __hip_bfloat16* __restrict__ x, const __hip_bfloat16* __restrict__ wo, const __hip_bfloat16* __restrict__ wd,
                                                 const char* __restrict__ ws, __hip_bfloat16* __restrict__ out) {
    __shared__ __attribute__((aligned(16))) char lds[K3_LDS];
    const int tid = threadIdx.x, l = tid & 63, wv = __builtin_amdgcn_readfirstlane(tid >> 6);
    const int b = blockIdx.x, xcd = b & 7, i = b >> 3, rb = (xcd >> 1) * 8 + (i & 7), cb = (xcd & 1) * 4 + (i >> 3);
    const unsigned lds0 = (unsigned)(uintptr_t)lds + wv * K3_RING * K3_STEP;
    const int nst = wv < 2 ? 19 : 18;
    unsigned wplain[5], wmain[5], wext[5];
#pragma unroll
    for (int j = 0; j < 5; ++j) {
        const int idx = j * 64 + l, row = idx / 10, ch = idx % 10;
        wplain[j] = (unsigned)(row * DM * 2 + ch * 16);
        wmain[j] = (unsigned)(((row >> 4) * 20 + (row & 15)) * DM * 2 + ch * 16);
        wext[j] = (unsigned)(((row >> 2) * 20 + 16 + (row & 3)) * DM * 2 + ch * 16);
    }
    const int g = l >> 4;
    const unsigned aoff = (unsigned)((g >> 1) * 1024 + ((g & 1) * 32 + (l & 15)) * 16);
    const char* ab = ws + WS_AF + (size_t)rb * 2 * NUNIT * 1024;
    const char* wob = (const char*)(wo + cb * 80);
    const char* wdb = (const char*)(wd + cb * 80);
    auto stage = [&](int it) {
        const int st = wv + 4 * it;
        const unsigned d = lds0 + (it & (K3_RING - 1)) * K3_STEP;
#pragma unroll
        for (int m = 0; m < 4; ++m) glds16(ab + (size_t)((m >> 1) * NUNIT + st * 2) * 1024 + (m & 1) * 256, aoff, d + m * 1024);
        if (st < 16) {
#pragma unroll
            for (int j = 0; j < 5; ++j) glds16(wob + (size_t)st * 40 * DM * 2, wmain[j], d + 4096 + j * 1024);
        } else if (st < 20) {
#pragma unroll
            for (int j = 0; j < 5; ++j) glds16(wob + (size_t)(st - 16) * 160 * DM * 2, wext[j], d + 4096 + j * 1024);
        } else {
#pragma unroll
            for (int j = 0; j < 5; ++j) glds16(wdb + (size_t)(st - 20) * 32 * DM * 2, wplain[j], d + 4096 + j * 1024);
        }
    };
    const int orow = rb * 64 + wv * 16 + (l & 15), ocol = cb * 80 + g * 4;
    u32x2 xr[5];
#pragma unroll
    for (int n = 0; n < 5; ++n) xr[n] = *(const u32x2*)(x + (size_t)orow * DM + ocol + n * 16);
    stage(0); stage(1); stage(2);
    const int wa = 4096 + ((g >> 1) * 16 + (g & 1) * 4 + ((l >> 2) & 3)) * 160 + (l & 3) * 8;
    f32x4 acc[4][5] = {};
    for (int it = 0; it < nst; ++it) {
        if (it + 3 < nst) { stage(it + 3); WAIT_VM(27); }
        else if (it + 2 < nst) { WAIT_VM(18); } else if (it + 1 < nst) { WAIT_VM(9); } else { WAIT_VM(0); }
        const lds_cptr sb = (lds_cptr)lds + wv * K3_RING * K3_STEP + (it & (K3_RING - 1)) * K3_STEP;
        bf16x8 af[4], wf[5];
#pragma unroll
        for (int m = 0; m < 4; ++m) af[m] = ldsr128(sb + m * 1024 + l * 16);
#pragma unroll
        for (int n = 0; n < 5; ++n) wf[n] = cat(ldstr(sb + wa + n * 32), ldstr(sb + wa + 8 * 160 + n * 32));
#pragma unroll
        for (int m = 0; m < 4; ++m)
#pragma unroll
            for (int n = 0; n < 5; ++n) acc[m][n] = MFMA16(wf[n], af[m], acc[m][n]);
        asm volatile("s_waitcnt lgkmcnt(0)" ::: "memory");
    }
    __syncthreads();
    f32x4* red = (f32x4*)lds;
#pragma unroll
    for (int m = 0; m < 4; ++m)
#pragma unroll
        for (int n = 0; n < 5; ++n) red[(wv * 20 + m * 5 + n) * 64 + l] = acc[m][n];
    __syncthreads();
#pragma unroll
    for (int n = 0; n < 5; ++n) {
        f32x4 s = red[(0 * 20 + wv * 5 + n) * 64 + l];
#pragma unroll
        for (int p = 1; p < 4; ++p) s += red[(p * 20 + wv * 5 + n) * 64 + l];
        s[0] += __uint_as_float(xr[n][0] << 16); s[1] += __uint_as_float(xr[n][0] & 0xffff0000u);
        s[2] += __uint_as_float(xr[n][1] << 16); s[3] += __uint_as_float(xr[n][1] & 0xffff0000u);
        u32x2 pk = {cvtpk(s[0], s[1]), cvtpk(s[2], s[3])};
        *(u32x2*)(out + (size_t)orow * DM + ocol + n * 16) = pk;
    }
}

extern "C" void kernel_launch(void* const* d_in, const int* in_sizes, int n_in, void* d_out, int out_size, void* d_ws, size_t ws_size,
                              hipStream_t stream) {
    const __hip_bfloat16* x = (const __hip_bfloat16*)d_in[0];
    const __hip_bfloat16* wqkv = (const __hip_bfloat16*)d_in[1];
    const __hip_bfloat16* wo = (const __hip_bfloat16*)d_in[2];
    const __hip_bfloat16* wg = (const __hip_bfloat16*)d_in[4];
    const __hip_bfloat16* wu = (const __hip_bfloat16*)d_in[5];
    const __hip_bfloat16* wd = (const __hip_bfloat16*)d_in[6];
    char* ws = (char*)d_ws;
    k1_qkv<<<256, 256, 0, stream>>>(x, wqkv, ws);
    k2_attn_gu<<<512, 256, 0, stream>>>(wg, wu, ws);
    k3_out<<<256, 256, 0, stream>>>(x, wo, wd, ws, (__hip_bfloat16*)d_out);
}
```

```cpp
#include <hip/hip_runtime.h>
#include <hip/hip_bf16.h>
#include <stdint.h>
#include <type_traits>

typedef __attribute__((ext_vector_type(8))) short bf16x8;
typedef __attribute__((ext_vector_type(4))) short s16x4;
typedef __attribute__((ext_vector_type(2))) __bf16 bf16x2;
typedef __attribute__((ext_vector_type(16))) float f32x16;
typedef __attribute__((ext_vector_type(4))) float f32x4;
typedef __attribute__((ext_vector_type(2))) float f32x2;
typedef __attribute__((ext_vector_type(4))) unsigned u32x4;
typedef __attribute__((ext_vector_type(2))) unsigned u32x2;
typedef unsigned long long u64;
typedef __attribute__((address_space(3))) const char* lds_cptr;
typedef __attribute__((address_space(3))) char* lds_ptr;
typedef __attribute__((address_space(1))) u64 gu64;

constexpr int SEQ = 2048, DM = 640, NH = 32, FF = 1728;
constexpr int NUNIT = 148;
constexpr size_t WS_EPOCH = 0;
constexpr size_t WS_FLN = 1024, WS_FQKV = WS_FLN + 2048, WS_FGU = WS_FQKV + 2048, WS_FAT = WS_FGU + 2048;
constexpr size_t WS_ST = 16384;
constexpr size_t WS_CS = WS_ST + 32768;
constexpr size_t WS_Q = 65536;
constexpr size_t WS_KV = WS_Q + (size_t)NH * 64 * 1280;
constexpr size_t WS_XN = WS_KV + (size_t)NH * 64 * 2560;
constexpr size_t WS_AF = WS_XN + (size_t)64 * 40 * 1024;
constexpr int LDS_BYTES = 81920;

#define SBAR() __builtin_amdgcn_sched_barrier(0)
#define PIN(x) asm volatile("" : "+v"(x))
#define MFMA32(a, b, c) __builtin_amdgcn_mfma_f32_32x32x16_bf16(a, b, c, 0, 0, 0)
#define MFMA32K8(a, b, c) __builtin_amdgcn_mfma_f32_32x32x8bf16_1k(a, b, c, 0, 0, 0)
#define MFMA16(a, b, c) __builtin_amdgcn_mfma_f32_16x16x32_bf16(a, b, c, 0, 0, 0)
#define STR_(x) #x
#define WAIT_BAR(N) asm volatile("s_waitcnt vmcnt(" STR_(N) ") lgkmcnt(0)\n\ts_barrier" ::: "memory")
#define WAIT_VM(N) asm volatile("s_waitcnt vmcnt(" STR_(N) ")" ::: "memory")
#define LGKM0() __builtin_amdgcn_s_waitcnt(0xc07f)
#define RLX_AGENT __ATOMIC_RELAXED, __HIP_MEMORY_SCOPE_AGENT
template <int N> __device__ __forceinline__ void wait_bar() {
    LGKM0(); asm volatile("s_waitcnt vmcnt(%0)\n\ts_barrier" ::"n"(N) : "memory"); }
template <int N> __device__ __forceinline__ void wait_vm() { asm volatile("s_waitcnt vmcnt(%0)" ::"n"(N) : "memory"); }

__device__ __forceinline__ unsigned cvtpk(float lo, float hi) {
    return __builtin_bit_cast(unsigned, __builtin_convertvector((f32x2{lo, hi}), bf16x2)); }
template <int LO> __device__ __forceinline__ void glds16(const void* sbase, unsigned voff, unsigned lds_base) {
    asm volatile("s_add_u32 m0, %2, %3\n\ts_nop 0\n\tglobal_load_lds_dwordx4 %0, %1" ::"v"(voff), "s"(sbase), "s"(lds_base), "n"(LO) : "memory", "scc"); }
template <int O> __device__ __forceinline__ void glds16c(const void* sbase, unsigned voff, unsigned lds_base) {
    asm volatile("s_mov_b32 m0, %2\n\ts_nop 0\n\tglobal_load_lds_dwordx4 %0, %1 offset:%3 sc1" ::"v"(voff), "s"(sbase), "s"(lds_base), "n"(O) : "memory"); }
template <int O = 0> __device__ __forceinline__ void gld16c(u32x4& d, const void* sbase, unsigned voff) {
    asm volatile("global_load_dwordx4 %0, %1, %2 offset:%3 sc1" : "=v"(d) : "v"(voff), "s"(sbase), "n"(O) : "memory"); }
template <int O = 0> __device__ __forceinline__ void gld8c(u32x2& d, const void* sbase, unsigned voff) {
    asm volatile("global_load_dwordx2 %0, %1, %2 offset:%3 sc1" : "=v"(d) : "v"(voff), "s"(sbase), "n"(O) : "memory"); }
template <class T> __device__ __forceinline__ void SPIN_(T& p) { asm volatile("" : "+s"(p)); }
template <class T> __device__ __forceinline__ void pinv(T& p) { asm volatile("" : "+v"(p)); }
template <int N, class F> __device__ __forceinline__ void sfor(F&& f) {
    if constexpr (N > 0) { sfor<N - 1>(f); f(std::integral_constant<int, N - 1>{}); } }
__device__ __forceinline__ void gst16c(void* sbase, unsigned voff, u32x4 v) {
    asm volatile("global_store_dwordx4 %0, %1, %2 sc1\n\ts_nop 1" ::"v"(voff), "v"(v), "s"(sbase) : "memory"); }
__device__ __forceinline__ void gst8c(void* sbase, unsigned voff, u32x2 v) {
    asm volatile("global_store_dwordx2 %0, %1, %2 sc1" ::"v"(voff), "v"(v), "s"(sbase) : "memory"); }
__device__ __forceinline__ bf16x8 ldsr128(lds_cptr p) { return *(const __attribute__((address_space(3))) bf16x8*)p; }
__device__ __forceinline__ s16x4 ldsr64(lds_cptr p) { return *(const __attribute__((address_space(3))) s16x4*)p; }
__device__ __forceinline__ s16x4 ldstr(lds_cptr p) {
    return __builtin_amdgcn_ds_read_tr16_b64_v4i16((__attribute__((address_space(3))) s16x4*)p); }
__device__ __forceinline__ bf16x8 cat(s16x4 a, s16x4 b) { return __builtin_shufflevector(a, b, 0, 1, 2, 3, 4, 5, 6, 7); }
__device__ __forceinline__ float dot2(unsigned a, unsigned b, float c) {
    return __builtin_amdgcn_fdot2_f32_bf16(__builtin_bit_cast(bf16x2, a), __builtin_bit_cast(bf16x2, b), c, false); }
__device__ __forceinline__ float xhalf(float v) { return __shfl_xor(v, 32); }
#define STAMP(slot)
constexpr unsigned ONES2 = 0x3f803f80u;
constexpr float LN_EPS = 1e-5f;
constexpr unsigned SPIN_CAP = 1u << 20;

__device__ __forceinline__ void publish(char* ws, size_t off, u64 tag) {
    WAIT_BAR(0);
    if (threadIdx.x == 0) __hip_atomic_store((gu64*)(ws + off), tag, RLX_AGENT);
}
__device__ __forceinline__ void await(char* ws, unsigned off0, unsigned off1, u64 tag) {
    if (threadIdx.x < 64) {
#pragma nounroll
        for (unsigned spin = 0; spin < SPIN_CAP; ++spin) {
            const u64 a = __hip_atomic_load((gu64*)(ws + off0), RLX_AGENT), b = __hip_atomic_load((gu64*)(ws + off1), RLX_AGENT);
            if (__all(a == tag && b == tag)) break;
            __builtin_amdgcn_s_sleep(2);
        }
    }
    asm volatile("s_barrier" ::: "memory");
}

constexpr int QX = 0, QW = 49152;

template <bool VT>
__device__ __forceinline__ void qkv_block(const __hip_bfloat16* __restrict__ x, const __hip_bfloat16* __restrict__ w,
                                          char* __restrict__ ws, char* lds, int rb, int cb, u64 epoch) {
    const int tid = threadIdx.x, l = tid & 63, wv = __builtin_amdgcn_readfirstlane(tid >> 6);
    const int wm = wv >> 1, wn = wv & 1, h = l >> 5, r32 = l & 31;
    const unsigned lds0 = (unsigned)(uintptr_t)lds;
    unsigned xo[4], wo_[2];
#pragma unroll
    for (int i = 0; i < 4; ++i) {
        const int r = 32 * wv + 8 * i + (l >> 3);
        xo[i] = (unsigned)(r * DM + (((l & 7) ^ ((r >> 1) & 7)) * 8)) * 2;
    }
#pragma unroll
    for (int i = 0; i < 2; ++i) {
        const int kr = 8 * wv + 4 * i + (l >> 4);
        wo_[i] = (unsigned)(kr * (3 * DM) + (((l & 15) ^ ((kr & 3) << 2)) * 8)) * 2;
    }
    const char* xb = (const char*)(x + (size_t)rb * 128 * DM);
    const char* wb = (const char*)(w + cb * 128);
    const unsigned lx = lds0 + QX + wv * 4096, lw = lds0 + QW + wv * 2048;
#define dma_x(kt, i) do { glds16<((kt) % 3) * 16384 + (i) * 1024>(xb, xo[i], lx); if ((i) == 3) { xb += 128; SPIN_(xb); } } while (0)
#define dma_w(hs, i) do { glds16<((hs) & 3) * 8192 + (i) * 1024>(wb, wo_[i], lw); if ((i) == 1) { wb += 32 * 3 * DM * 2; SPIN_(wb); } } while (0)
    dma_x(0, 0); dma_x(0, 1); dma_x(0, 2); dma_x(0, 3); dma_w(0, 0); dma_w(0, 1);
    dma_w(1, 0); dma_w(1, 1); dma_x(1, 0); dma_x(1, 1);
    dma_w(2, 0); dma_w(2, 1); dma_x(1, 2); dma_x(1, 3);
    int xa[4], wa[2];
#pragma unroll
    for (int ks = 0; ks < 4; ++ks) xa[ks] = QX + (wm * 64 + r32) * 128 + (((2 * ks + h) ^ ((r32 >> 1) & 7)) * 16);
    {
        const int gi = l >> 4, p = l & 3, q = (l >> 2) & 3;
#pragma unroll
        for (int nt = 0; nt < 2; ++nt) {
            const int ch = wn * 8 + nt * 4 + (gi & 1) * 2 + (p >> 1);
            wa[nt] = QW + ((gi >> 1) * 8 + q) * 256 + ((ch ^ (q << 2)) * 16) + (p & 1) * 8;
        }
    }
    const unsigned so = (unsigned)(WS_ST + (rb * 256 + tid) * 8), co = (unsigned)(WS_CS + (cb * 128 + (tid & 127)) * 8);
    u32x2 gs, gc;
    f32x16 acc[2][2] = {};
    bf16x8 xf[2][2], wf[2][2];
    sfor<41>([&](auto S_) {
        constexpr int s = S_.value, hs = s >> 1, kt = s >> 2, cur = s & 1, prv = cur ^ 1;
        if (s < 40 && cur == 0) {
            if (hs < 17) wait_bar<8>(); else if (hs == 17) wait_bar<8>(); else if (hs == 18) wait_bar<4>(); else wait_bar<0>();
            if (hs == 0) STAMP(1);
        } else LGKM0();
        SBAR();
        if (s > 0) acc[0][0] = VT ? MFMA32(xf[prv][0], wf[prv][0], acc[0][0]) : MFMA32(wf[prv][0], xf[prv][0], acc[0][0]);
        SBAR();
        if (s < 40) {
            const lds_cptr xs = (lds_cptr)lds + (kt % 3) * 16384 + xa[s & 3];
            const lds_cptr wp = (lds_cptr)lds + (hs & 3) * 8192 + cur * 4096;
            xf[cur][0] = ldsr128(xs);
            wf[cur][0] = cat(ldstr(wp + wa[0]), ldstr(wp + wa[0] + 1024));
            wf[cur][1] = cat(ldstr(wp + wa[1]), ldstr(wp + wa[1] + 1024));
            xf[cur][1] = ldsr128(xs + 4096);
        }
        SBAR();
        if (s > 0) acc[0][1] = VT ? MFMA32(xf[prv][0], wf[prv][1], acc[0][1]) : MFMA32(wf[prv][1], xf[prv][0], acc[0][1]);
        SBAR();
        if (s == 32) { gld8c(gs, ws, so); gld8c(gc, ws, co); }
        if (cur == 0) { if (hs + 3 < 20) dma_w(hs + 3, 0); } else if (kt + 2 < 10) dma_x(kt + 2, (hs & 1) * 2);
        SBAR();
        if (s > 0) acc[1][0] = VT ? MFMA32(xf[prv][1], wf[prv][0], acc[1][0]) : MFMA32(wf[prv][0], xf[prv][1], acc[1][0]);
        SBAR();
        if (cur == 0) { if (hs + 3 < 20) dma_w(hs + 3, 1); } else if (kt + 2 < 10) dma_x(kt + 2, (hs & 1) * 2 + 1);
        SBAR();
        if (s > 0) acc[1][1] = VT ? MFMA32(xf[prv][1], wf[prv][1], acc[1][1]) : MFMA32(wf[prv][1], xf[prv][1], acc[1][1]);
        SBAR();
    });
    STAMP(2);
    const unsigned tag = (unsigned)(epoch * 0x9E3779B97F4A7C15ull + 1);
    asm volatile("" : "+v"(gs), "+v"(gc));
#pragma nounroll
    for (unsigned spin = 0; spin < SPIN_CAP && !__all(gs[1] == tag && gc[1] == tag); ++spin) {
        gld8c(gs, ws, so); gld8c(gc, ws, co);
        asm volatile("s_waitcnt vmcnt(0)" : "+v"(gs), "+v"(gc)::"memory");
    }
    float* st = (float*)(lds + QW);
    st[tid] = __uint_as_float(gs[0]);
    if (tid < 128) st[256 + tid] = __uint_as_float(gc[0]);
    __syncthreads();
    if (!VT) {
        const bool isq = cb < 5;
        const float osc = isq ? 0.22360679774997896f * 1.4426950408889634f : 1.f;
        const int g0 = (isq ? cb : cb - 5) * 16 + wn * 8;
        const lds_ptr sg = (lds_ptr)lds + 16384 + wv * 8192;
#pragma unroll
        for (int mt = 0; mt < 2; ++mt) {
            const f32x2 mr = *(const f32x2*)(st + (wm * 64 + mt * 32 + r32) * 2);
            const float mu = mr[0], rs = mr[1] * osc;
#pragma unroll
            for (int g = 0; g < 8; ++g) {
                const f32x4 c4 = *(const f32x4*)(st + 256 + wn * 64 + g * 8 + 4 * h);
                float v[4];
#pragma unroll
                for (int j = 0; j < 4; ++j) v[j] = (acc[mt][g >> 2][(g & 3) * 4 + j] - mu * c4[j]) * rs;
                const bool split = (g0 + g) % 5 == 2;
                const int o = split ? l * 8 : r32 * 16 + h * 8;
                *(__attribute__((address_space(3))) u32x2*)(sg + (mt * 8 + g) * 512 + o) = u32x2{cvtpk(v[0], v[1]), cvtpk(v[2], v[3])};
            }
        }
        char* ob = ws + (isq ? WS_Q : WS_KV);
        const int tstr = isq ? 1280 : 2560;
        u32x4 v[8]; unsigned oo[4];
#pragma unroll
        for (int p = 0; p < 4; ++p) {
            const int G = g0 + 2 * p + h, P = G / 5, r = G % 5;
            const int head = 2 * P + (r > 2 || (r == 2 && r32 >= 16));
            const int o = r == 2 ? 1024 + (r32 & 15) * 16 : (r > 2 ? r - 3 : r) * 512 + r32 * 16;
            oo[p] = (unsigned)((head * 64 + rb * 4 + wm * 2) * tstr + o);
#pragma unroll
            for (int mt = 0; mt < 2; ++mt) v[2 * p + mt] = *(const __attribute__((address_space(3))) u32x4*)(sg + (mt * 8 + 2 * p) * 512 + l * 16);
        }
#pragma unroll
        for (int p = 0; p < 8; ++p) gst16c(ob + (p & 1) * tstr, oo[p >> 1], v[p]);
    } else {
#pragma unroll
        for (int mt = 0; mt < 2; ++mt) {
            const int tile = rb * 4 + wm * 2 + mt;
            f32x4 mr[8];
#pragma unroll
            for (int rg = 0; rg < 4; ++rg) {
                mr[2 * rg] = *(const f32x4*)(st + (wm * 64 + mt * 32 + 8 * rg + 4 * h) * 2);
                mr[2 * rg + 1] = *(const f32x4*)(st + (wm * 64 + mt * 32 + 8 * rg + 4 * h) * 2 + 4);
            }
#pragma unroll
            for (int nt = 0; nt < 2; ++nt) {
                const int fl = wn * 64 + nt * 32 + r32, f = (cb - 10) * 128 + fl, head = f / 20, d = f % 20;
                const float c = st[256 + fl];
#pragma unroll
                for (int s = 0; s < 2; ++s) {
                    float v[8];
#pragma unroll
                    for (int j = 0; j < 8; ++j)
                        v[j] = (acc[mt][nt][8 * s + j] - mr[4 * s + (j >> 1)][(j & 1) * 2] * c) * mr[4 * s + (j >> 1)][(j & 1) * 2 + 1];
                    gst16c(ws + WS_KV, (unsigned)((head * 64 + tile) * 2560 + 1280 + ((s * 2 + h) * 20 + d) * 16),
                           u32x4{cvtpk(v[0], v[1]), cvtpk(v[2], v[3]), cvtpk(v[4], v[5]), cvtpk(v[6], v[7])});
                }
            }
        }
    }
}

__device__ __forceinline__ void colsum_block(const __hip_bfloat16* __restrict__ w, char* __restrict__ ws, char* lds, int cb, u64 epoch) {
    const int tid = threadIdx.x, c = tid & 15, rg = tid >> 4;
    const u32x4* src = (const u32x4*)(w + (size_t)rg * 3 * DM + cb * 128 + c * 8);
    u32x4 v[40];
#pragma unroll
    for (int i = 0; i < 40; ++i) v[i] = src[(size_t)i * 16 * 3 * DM / 8];
    float s[8] = {};
    unsigned pick_lo = 0x00003f80u, pick_hi = 0x3f800000u;
    pinv(pick_lo); pinv(pick_hi);
#pragma unroll
    for (int i = 0; i < 40; ++i)
#pragma unroll
        for (int j = 0; j < 4; ++j) { s[2 * j] = dot2(v[i][j], pick_lo, s[2 * j]); s[2 * j + 1] = dot2(v[i][j], pick_hi, s[2 * j + 1]); }
    float* red = (float*)lds;
    *(f32x4*)(red + rg * 128 + c * 8) = f32x4{s[0], s[1], s[2], s[3]};
    *(f32x4*)(red + rg * 128 + c * 8 + 4) = f32x4{s[4], s[5], s[6], s[7]};
    __syncthreads();
    if (tid < 128) {
        float t = 0.f;
#pragma unroll
        for (int i = 0; i < 16; ++i) t += red[i * 128 + tid];
        gst8c(ws, (unsigned)(WS_CS + (cb * 128 + tid) * 8), u32x2{__float_as_uint(t), (unsigned)(epoch * 0x9E3779B97F4A7C15ull + 1)});
    }
    __syncthreads();
}

constexpr int AT_PAIR = 5152, AT_CHUNK = 4 * AT_PAIR;
constexpr float SHIFT_MARGIN = 64.f;

#define EXP4(P, S, o) do { \
    float e0 = __builtin_amdgcn_exp2f(S[o + 0]), e1 = __builtin_amdgcn_exp2f(S[o + 1]); \
    float e2 = __builtin_amdgcn_exp2f(S[o + 2]), e3 = __builtin_amdgcn_exp2f(S[o + 3]); \
    P[((o) >> 1) & 3] = cvtpk(e0, e1); P[(((o) >> 1) & 3) + 1] = cvtpk(e2, e3); } while (0)
#define GRP(S, o, P, j) do { \
    const float n0 = __builtin_amdgcn_exp2f(S[o + 0]); SBAR(); P[j] = cvtpk(e0, e1); SBAR(); \
    const float n1 = __builtin_amdgcn_exp2f(S[o + 1]); SBAR(); P[j + 1] = cvtpk(e2, e3); SBAR(); \
    e0 = n0; e1 = n1; e2 = __builtin_amdgcn_exp2f(S[o + 2]); SBAR(); e3 = __builtin_amdgcn_exp2f(S[o + 3]); } while (0)

__device__ __forceinline__ float max16(const f32x16& s) {
    float a = fmaxf(fmaxf(s[0], s[1]), s[2]), b = fmaxf(fmaxf(s[3], s[4]), s[5]);
    a = fmaxf(fmaxf(a, s[6]), s[7]); b = fmaxf(fmaxf(b, s[8]), s[9]);
    a = fmaxf(fmaxf(a, s[10]), s[11]); b = fmaxf(fmaxf(b, s[12]), s[13]);
    return fmaxf(fmaxf(a, s[14]), fmaxf(b, s[15]));
}
__device__ __forceinline__ f32x16 splat16(float v) { f32x16 r;
#pragma unroll
    for (int i = 0; i < 16; ++i) r[i] = v;
    return r; }

__device__ __forceinline__ f32x16 attn_fallback(const char* kv, bf16x8 q1, s16x4 q2, int l) {
    const int h = l >> 5, r32 = l & 31, d = r32 < 20 ? r32 : 0;
    float m = -INFINITY; f32x16 o = {};
    const f32x16 z = {};
    for (int t = 0; t < 64; ++t) {
        u32x4 k1, v0, v1; u32x2 k2;
        gld16c(k1, kv, t * 2560 + l * 16); gld8c(k2, kv, t * 2560 + 1024 + r32 * 8);
        gld16c(v0, kv, t * 2560 + 1280 + (h * 20 + d) * 16); gld16c(v1, kv, t * 2560 + 1280 + ((2 + h) * 20 + d) * 16);
        asm volatile("s_waitcnt vmcnt(0)" : "+v"(k1), "+v"(k2), "+v"(v0), "+v"(v1)::"memory");
        f32x16 s = MFMA32(__builtin_bit_cast(bf16x8, k1), q1, z); s = MFMA32K8(__builtin_bit_cast(s16x4, k2), q2, s);
        float tm = max16(s); tm = fmaxf(tm, xhalf(tm));
        const float mn = fmaxf(m, tm), al = __builtin_amdgcn_exp2f(m - mn); m = mn;
        u32x4 p0, p1;
#pragma unroll
        for (int i = 0; i < 16; ++i) { s[i] -= mn; o[i] *= al; }
        EXP4(p0, s, 0); EXP4(p0, s, 4); EXP4(p1, s, 8); EXP4(p1, s, 12);
        const u32x4 ones = {ONES2, ONES2, ONES2, ONES2};
        if (r32 >= 20) { v0 = ones; v1 = ones; }
        o = MFMA32(__builtin_bit_cast(bf16x8, v0), __builtin_bit_cast(bf16x8, p0), o);
        o = MFMA32(__builtin_bit_cast(bf16x8, v1), __builtin_bit_cast(bf16x8, p1), o);
    }
    return o;
}

__device__ __forceinline__ void attn_finish(f32x16 o, const char* kv, bf16x8 q1, s16x4 q2, char* ws, int head, int rt, int l) {
    const int h = l >> 5, r32 = l & 31;
    bool ok = true;
#pragma unroll
    for (int i = 0; i < 12; ++i) ok = ok && (fabsf(o[i]) < 1e37f);
    if (__builtin_expect(__any(!ok), 0)) o = attn_fallback(kv, q1, q2, l);
    const float lx = xhalf(o[8]), inv = 1.f / (h ? o[8] : lx);
    char* dst = ws + WS_AF + (size_t)rt * NUNIT * 1024;
    gst16c(dst, head * 1024 + l * 16,
           u32x4{cvtpk(o[0] * inv, o[1] * inv), cvtpk(o[2] * inv, o[3] * inv), cvtpk(o[4] * inv, o[5] * inv), cvtpk(o[6] * inv, o[7] * inv)});
    if (h == 0) gst8c(dst, 32768 + head * 256 + r32 * 8, u32x2{cvtpk(o[8] * inv, o[9] * inv), cvtpk(o[10] * inv, o[11] * inv)});
}

__device__ __forceinline__ void attn_block(char* ws, char* lds, int head, int qb, u64 tag) {
    const int tid = threadIdx.x, l = tid & 63, wv = __builtin_amdgcn_readfirstlane(tid >> 6), h = l >> 5, r32 = l & 31;
    const unsigned lds0 = (unsigned)(uintptr_t)lds;
    const char* kv = ws + WS_KV + (size_t)head * 64 * 2560;
    const unsigned voff = l * 16;
    if (tid < 96) *(__attribute__((address_space(3))) unsigned*)((lds_ptr)lds + (tid >> 3) * AT_PAIR + 5120 + (tid & 7) * 4) = (tid & 4) ? 0u : ONES2;
    {
        const int c0 = head * 20 >> 7, c1 = (head * 20 + 19) >> 7;
        await(ws, (unsigned)WS_FQKV + ((l & 32 ? 10 : 5) + (l & 16 ? c1 : c0)) * 128 + (l & 15) * 8,
              (unsigned)WS_FQKV + (l & 2 ? c1 : c0) * 128 + (2 * qb + (l & 1)) * 8, tag);
    }
    STAMP(4);
#define dma_kv(c, slot, i) glds16c<((i) < 2 ? (i) : (i) - 2) * 1024>(kv + ((c) * 4 + wv) * 5120 + ((i) < 2 ? 0 : 2048), voff, \
                                                                 lds0 + (slot) * AT_CHUNK + wv * AT_PAIR + ((i) < 2 ? 0 : 2048))
    const int rt0 = qb * 8 + wv * 2;
    const char* qp = ws + WS_Q + (size_t)(head * 64 + rt0) * 1280;
    u32x4 qa1_, qb1_; u32x2 qa2_, qb2_;
    gld16c(qa1_, qp, voff); gld16c<1280>(qb1_, qp, voff); gld8c<1024>(qa2_, qp, r32 * 8); gld8c<2304>(qb2_, qp, r32 * 8);
    dma_kv(0, 0, 0); dma_kv(0, 0, 1); dma_kv(0, 0, 2); dma_kv(0, 0, 3); dma_kv(0, 0, 4); dma_kv(1, 1, 0); dma_kv(1, 1, 1);
    const int ka1 = l * 16, ka2 = 1024 + r32 * 8;
    int va[2][2];
#pragma unroll
    for (int tp = 0; tp < 2; ++tp)
#pragma unroll
        for (int s = 0; s < 2; ++s) va[tp][s] = r32 < 20 ? tp * 2560 + 1280 + ((s * 2 + h) * 20 + r32) * 16 : (r32 == 20 ? 5120 : 5136);
    asm volatile("s_waitcnt vmcnt(2) lgkmcnt(0)\n\ts_barrier" : "+v"(qa1_), "+v"(qb1_), "+v"(qa2_), "+v"(qb2_)::"memory");
    if (h) { qa2_ = u32x2{0, 0}; qb2_ = u32x2{0, 0}; }
    const bf16x8 qa1 = __builtin_bit_cast(bf16x8, qa1_), qb1 = __builtin_bit_cast(bf16x8, qb1_);
    const s16x4 qa2 = __builtin_bit_cast(s16x4, qa2_), qb2 = __builtin_bit_cast(s16x4, qb2_);
    STAMP(5);
    f32x16 nma, nmb;
    {
        const f32x16 z = {};
        float ma = -INFINITY, mb = -INFINITY;
#pragma unroll
        for (int t = 0; t < 4; ++t) {
            const lds_cptr p = (lds_cptr)lds + (t >> 1) * AT_PAIR + (t & 1) * 2560;
            const bf16x8 k1 = ldsr128(p + ka1); const s16x4 k2 = ldsr64(p + ka2);
            f32x16 sa = MFMA32(k1, qa1, z), sb = MFMA32(k1, qb1, z);
            sa = MFMA32K8(k2, qa2, sa); sb = MFMA32K8(k2, qb2, sb);
            ma = fmaxf(ma, max16(sa)); mb = fmaxf(mb, max16(sb));
        }
        ma = fmaxf(ma, xhalf(ma)); mb = fmaxf(mb, xhalf(mb));
        nma = splat16(-(ma + SHIFT_MARGIN)); nmb = splat16(-(mb + SHIFT_MARGIN));
        PIN(nma); PIN(nmb);
    }
    f32x16 oa = {}, ob = {}, sa, sb;
    u32x4 pa0, pa1, pb0 = {}, pb1 = {};
    bf16x8 k1, v0 = {}, v1 = {}; s16x4 k2;
    k1 = ldsr128((lds_cptr)lds + ka1); k2 = ldsr64((lds_cptr)lds + ka2);
    sa = MFMA32(k1, qa1, nma); sa = MFMA32K8(k2, qa2, sa);
    float e0 = 0.f, e1 = 0.f, e2 = 0.f, e3 = 0.f;
    int slot = 0;
    for (int c = 0; c < 8; ++c) {
        const lds_cptr cb = (lds_cptr)lds + slot * AT_CHUNK;
        const int nslot = slot == 2 ? 0 : slot + 1, pslot = slot == 0 ? 2 : slot - 1;
        const lds_cptr nb = (lds_cptr)lds + nslot * AT_CHUNK;
        const int cn = (c + 1) & 7, cn2 = (c + 2) & 7;
#pragma unroll
        for (int t = 0; t < 8; ++t) {
            const lds_cptr kp = t < 7 ? cb + ((t + 1) >> 1) * AT_PAIR + ((t + 1) & 1) * 2560 : nb;
            const lds_cptr vp = cb + (t >> 1) * AT_PAIR;
            SBAR();
            sb = MFMA32(k1, qb1, nmb); SBAR();
            if (t == 7) { asm volatile("s_waitcnt vmcnt(0)\n\ts_barrier" ::: "memory"); SBAR(); }
            const bf16x8 nk1 = ldsr128(kp + ka1); const s16x4 nk2 = ldsr64(kp + ka2);
            const bf16x8 nv0 = ldsr128(vp + va[t & 1][0]), nv1 = ldsr128(vp + va[t & 1][1]);
            SBAR();
            GRP(sa, 0, pb1, 2); SBAR();
            sb = MFMA32K8(k2, qb2, sb); SBAR();
            GRP(sa, 4, pa0, 0); SBAR();
            ob = MFMA32(v0, __builtin_bit_cast(bf16x8, pb0), ob); SBAR();
            if (t == 7) dma_kv(cn2, pslot, 0); else if (t == 0) dma_kv(cn, nslot, 2); else if (t == 1) dma_kv(cn, nslot, 4);
            SBAR();
            GRP(sa, 8, pa0, 2); SBAR();
            ob = MFMA32(v1, __builtin_bit_cast(bf16x8, pb1), ob); SBAR();
            LGKM0(); SBAR();
            GRP(sa, 12, pa1, 0); SBAR();
            k1 = nk1; k2 = nk2; v0 = nv0; v1 = nv1;
            sa = MFMA32(k1, qa1, nma); SBAR();
            GRP(sb, 0, pa1, 2); SBAR();
            sa = MFMA32K8(k2, qa2, sa); SBAR();
            GRP(sb, 4, pb0, 0); SBAR();
            oa = MFMA32(v0, __builtin_bit_cast(bf16x8, pa0), oa); SBAR();
            if (t == 7) dma_kv(cn2, pslot, 1); else if (t == 0) dma_kv(cn, nslot, 3);
            SBAR();
            GRP(sb, 8, pb0, 2); SBAR();
            oa = MFMA32(v1, __builtin_bit_cast(bf16x8, pa1), oa); SBAR();
            GRP(sb, 12, pb1, 0); SBAR();
        }
        slot = nslot;
    }
    pb1[2] = cvtpk(e0, e1); pb1[3] = cvtpk(e2, e3);
    ob = MFMA32(v0, __builtin_bit_cast(bf16x8, pb0), ob);
    ob = MFMA32(v1, __builtin_bit_cast(bf16x8, pb1), ob);
    STAMP(6);
    attn_finish(oa, kv, qa1, qa2, ws, head, rt0, l);
    attn_finish(ob, kv, qb1, qb2, ws, head, rt0 + 1, l);
    publish(ws, WS_FAT + (qb * 32 + head) * 8, tag);
}

#define PRIO_A 1
#define PRIO_GU 1
#define PRIO_EP 2
#define PRIO_DN 1
__device__ __forceinline__ void gld16(u32x4& d, const void* sbase, unsigned voff) {
    asm volatile("global_load_dwordx4 %0, %1, %2" : "=v"(d) : "v"(voff), "s"(sbase) : "memory"); }
template <int O> __device__ __forceinline__ void gld8(u32x2& d, const void* sbase, unsigned voff) {
    asm volatile("global_load_dwordx2 %0, %1, %2 offset:%3" : "=v"(d) : "v"(voff), "s"(sbase), "n"(O) : "memory"); }
template <int LO, bool HALF> __device__ __forceinline__ void glds16v(const char* src, unsigned lds_base) {
    if (HALF) asm volatile("s_add_u32 m0, %1, %2\n\ts_mov_b32 exec_hi, 0\n\tglobal_load_lds_dwordx4 %0, off\n\ts_mov_b32 exec_hi, -1"
                           ::"v"(src), "s"(lds_base), "n"(LO) : "memory", "scc");
    else asm volatile("s_add_u32 m0, %1, %2\n\ts_nop 0\n\tglobal_load_lds_dwordx4 %0, off" ::"v"(src), "s"(lds_base), "n"(LO) : "memory", "scc"); }
__device__ __forceinline__ constexpr int gu_slot(int kt) { return (kt & 3) == 3 ? 65536 : (kt & 3) * 14336; }

__device__ __forceinline__ void role_b(const __hip_bfloat16* __restrict__ x, const __hip_bfloat16* __restrict__ wo,
                                       const __hip_bfloat16* __restrict__ wg, const __hip_bfloat16* __restrict__ wu,
                                       const __hip_bfloat16* __restrict__ wd, char* ws, __hip_bfloat16* __restrict__ out,
                                       char* lds, int j, u64 epoch) {
    const int tid = threadIdx.x, l = tid & 63, wv = __builtin_amdgcn_readfirstlane(tid >> 6);
    const unsigned lds0 = (unsigned)(uintptr_t)lds;
    const int xcd = j & 7, bi = j >> 3;
    __builtin_amdgcn_s_setprio(3);
    const int rot = wv * 3 - (wv == 3);
    u32x4 xv[10];
#pragma unroll
    for (int i = 0; i < 10; ++i) {
        const int gq = rot + i < 10 ? rot + i : rot + i - 10;
        gld16(xv[i], (const char*)x + (size_t)j * 8 * DM * 2 + gq * 128, (l & 7) * DM * 2 + (l >> 3) * 16);
    }
    const int rb = (xcd >> 2) * 8 + (bi & 7), cbk = (xcd & 3) * 4 + (bi >> 3);
    const int u0 = (cbk * 27) >> 2, rt = rb * 4 + wv;
    const char* wlo = (const char*)(wg < wu ? wg : wu);
    const size_t dg = (const char*)wg - wlo, du = (const char*)wu - wlo;
    const char* wsrc[4];
#pragma unroll
    for (int i = 0; i < 4; ++i) {
        const int idx = wv * 224 + i * 64 + (i == 3 ? (l & 31) : l), row = idx / 28, ch = idx % 28;
        wsrc[i] = wlo + (ch < 14 ? dg : du) + (size_t)row * FF * 2 + u0 * 32 + (ch < 14 ? ch : ch - 14) * 16;
    }
    const unsigned lg = lds0 + wv * 3584;
#define dma_g(kt, i) do { glds16v<gu_slot(kt) + (i) * 1024, (i) == 3>(wsrc[i], lg); wsrc[i] += 32 * FF * 2; } while (0)
    sfor<12>([&](auto I) { dma_g(I.value >> 2, I.value & 3); });
    asm volatile("" : "+s"(epoch));
    const u64 tag = epoch * 0x9E3779B97F4A7C15ull + 1;
    asm volatile("s_waitcnt vmcnt(12)" : "+v"(xv[0]), "+v"(xv[1]), "+v"(xv[2]), "+v"(xv[3]), "+v"(xv[4]), "+v"(xv[5]), "+v"(xv[6]),
                 "+v"(xv[7]), "+v"(xv[8]), "+v"(xv[9])::"memory");
    {
        float s1 = 0.f, s2 = 0.f;
#pragma unroll
        for (int i = 0; i < 10; ++i)
#pragma unroll
            for (int q = 0; q < 4; ++q) { s1 = dot2(xv[i][q], ONES2, s1); s2 = dot2(xv[i][q], xv[i][q], s2); }
#pragma unroll
        for (int o = 8; o < 64; o <<= 1) { s1 += __shfl_xor(s1, o); s2 += __shfl_xor(s2, o); }
        const float mu = s1 * (1.f / DM), rs = rsqrtf(fmaxf(s2 * (1.f / DM) - mu * mu, 0.f) + LN_EPS), nb = -mu * rs;
        if (tid < 8) gst16c(ws, (unsigned)WS_ST + (j * 8 + tid) * 16, u32x4{__float_as_uint(mu), (unsigned)tag, __float_as_uint(rs), (unsigned)tag});
        char* dst = ws + WS_XN + (size_t)(j >> 2) * 40 * 1024;
#pragma unroll
        for (int i = 0; i < 3; ++i)
            if (i < 2 || wv < 2) {
                u32x4 o;
#pragma unroll
                for (int q = 0; q < 4; ++q) {
                    const float lo = __uint_as_float(xv[i][q] << 16), hi = __uint_as_float(xv[i][q] & 0xffff0000u);
                    o[q] = cvtpk(fmaf(lo, rs, nb), fmaf(hi, rs, nb));
                }
                gst16c(dst, (unsigned)(((rot + i) * 8 + (l >> 3)) * 512 + ((j & 3) * 8 + (l & 7)) * 16), o);
            }
    }
    f32x16 acc[7] = {};
    bf16x8 wf[2][7] = {};
    u32x4 xr[4][2];
    xr[3][1] = u32x4{0, 0, 0, 0};
    sfor<7>([&](auto U) { pinv(acc[U.value]); pinv(wf[1][U.value]); }); pinv(xr[3][1]);
    publish(ws, WS_FLN + j * 8, tag);
    STAMP(1);
    await(ws, (unsigned)WS_FLN + (rb * 16 + (l & 15)) * 8, (unsigned)WS_FLN + (rb * 16 + (l & 15)) * 8, tag);
    STAMP(2);
    __builtin_amdgcn_s_setprio(PRIO_GU);
    const char* xsrc = ws + WS_XN + (size_t)rt * 40 * 1024;
    unsigned xoff = l * 16;
#pragma unroll
    for (int kt = 0; kt < 3; ++kt) { gld16c(xr[kt][0], xsrc, xoff); gld16c<1024>(xr[kt][1], xsrc, xoff); xoff += 2048; }
    const int rb2 = (xcd >> 1) * 8 + (bi & 7), cb2 = (xcd & 1) * 4 + (bi >> 3);
    const int g = l >> 4;
    unsigned wplain[5];
#pragma unroll
    for (int q = 0; q < 5; ++q) { const int idx = q * 64 + l; wplain[q] = (unsigned)((idx / 10) * DM * 2 + (idx % 10) * 16); }
    const char* wp = (const char*)(wd + cb2 * 80) + wv * 32 * DM * 2;
    const unsigned ld = lds0 + wv * 5120;
#define dma_d(s, q) do { \
        if ((s) == 14) wplain[q] += ((((q) * 64 + l) / 10) >> 4) * 4 * DM * 2; \
        if ((s) == 18) wplain[q] += (((((q) * 64 + l) / 10) >> 2) * 16 + 16 - ((((q) * 64 + l) / 10) >> 4) * 4) * DM * 2; \
        glds16<((s) & 3) * 20480 + (q) * 1024>(wp, wplain[q], ld); \
        if ((q) == 4) { \
            if ((s) < 12) wp += 128 * DM * 2; else if ((s) == 12) wp += wv < 2 ? 128 * DM * 2 : 0; \
            else if ((s) == 13) wp = (const char*)(wo + cb2 * 80) + wv * 160 * DM * 2; \
            else if ((s) < 17) wp += 40 * DM * 2; else if ((s) == 17) wp -= 120 * DM * 2; \
            SPIN_(wp); } } while (0)
    const int wa = ((g >> 1) * 8 + ((l >> 2) & 3)) * 448 + (g & 1) * 224 + (l & 3) * 8;
    {
        asm volatile("s_waitcnt vmcnt(2)" ::: "memory");
        auto gu_step = [&](auto K4, auto LAST) {
            constexpr int k4 = K4.value; constexpr bool last = LAST.value;
            constexpr bool more = !last || k4 == 0;
            wait_bar<(last && k4 == 2) ? 6 : (last && k4 == 3) ? 0 : 12>();
            pinv(xr[k4][0]); pinv(xr[k4][1]);
            const lds_cptr sb = (lds_cptr)lds + gu_slot(k4) + wa;
            sfor<7>([&](auto U) {
                constexpr int u = U.value;
                SBAR();
                acc[u] = MFMA32(wf[1][u], __builtin_bit_cast(bf16x8, xr[(k4 + 3) & 3][1]), acc[u]);
                SBAR();
                wf[0][u] = cat(ldstr(sb + u * 32), ldstr(sb + 4 * 448 + u * 32));
                if constexpr (more && u >= 1 && u < 5) dma_g(k4 + 3, u - 1);
                if constexpr (last && k4 == 3) dma_d(u / 5, u % 5);
            });
            SBAR(); LGKM0();
            sfor<7>([&](auto U) {
                constexpr int u = U.value;
                SBAR();
                acc[u] = MFMA32(wf[0][u], __builtin_bit_cast(bf16x8, xr[k4][0]), acc[u]);
                SBAR();
                wf[1][u] = cat(ldstr(sb + 16 * 448 + u * 32), ldstr(sb + 20 * 448 + u * 32));
                if (more && u == 1) gld16c(xr[(k4 + 3) & 3][0], xsrc, xoff);
                if (more && u == 3) { gld16c<1024>(xr[(k4 + 3) & 3][1], xsrc, xoff); xoff += 2048; }
                if constexpr (last && k4 == 3) dma_d((u + 7) / 5, (u + 7) % 5);
            });
            SBAR();
        };
        using std::integral_constant;
        for (int it = 0; it < 4; ++it) {
            gu_step(integral_constant<int, 0>{}, std::false_type{}); gu_step(integral_constant<int, 1>{}, std::false_type{});
            gu_step(integral_constant<int, 2>{}, std::false_type{}); gu_step(integral_constant<int, 3>{}, std::false_type{});
        }
        gu_step(integral_constant<int, 0>{}, std::true_type{}); gu_step(integral_constant<int, 1>{}, std::true_type{});
        gu_step(integral_constant<int, 2>{}, std::true_type{}); gu_step(integral_constant<int, 3>{}, std::true_type{});
        dma_d(2, 4);
#pragma unroll
        for (int u = 0; u < 7; ++u) acc[u] = MFMA32(wf[1][u], __builtin_bit_cast(bf16x8, xr[3][1]), acc[u]);
    }
    STAMP(3);
    __builtin_amdgcn_s_setprio(PRIO_EP);
    {
        char* dst = ws + WS_AF + ((size_t)rt * NUNIT + 40 + u0) * 1024;
#pragma unroll
        for (int u = 0; u < 7; ++u) {
            float hv[8];
#pragma unroll
            for (int q = 0; q < 8; ++q) {
                const float gt = acc[u][q];
                const float e = __builtin_amdgcn_exp2f(gt * fmaf(gt * gt, -0.1029432397f, -2.302208198f));
                hv[q] = gt * __builtin_amdgcn_rcpf(1.f + e) * acc[u][8 + q];
            }
            gst16c(dst, u * 1024 + l * 16, u32x4{cvtpk(hv[0], hv[1]), cvtpk(hv[2], hv[3]), cvtpk(hv[4], hv[5]), cvtpk(hv[6], hv[7])});
        }
    }
    publish(ws, WS_FGU + (rb * 16 + cbk) * 8, tag);
    STAMP(4);
    sfor<5>([&](auto Q) { dma_d(3, Q.value); });
    await(ws, (unsigned)WS_FGU + ((rb2 >> 1) * 16 + (l & 15)) * 8, (unsigned)WS_FGU + ((rb2 >> 1) * 16 + (l & 15)) * 8, tag);
    STAMP(5);
    __builtin_amdgcn_s_setprio(PRIO_DN);
    const char* ab = ws + WS_AF + (size_t)rb2 * 2 * NUNIT * 1024;
    const unsigned aoff = (unsigned)((g >> 1) * 1024 + ((g & 1) * 32 + (l & 15)) * 16);
    u32x4 af[4][4]; u32x2 ae[4][2];
    const unsigned aoff2 = aoff + NUNIT * 1024;
    const char* ap = ab + (40 + 2 * wv) * 1024;
#define ld_a(s, m) do { \
        gld16c<((m) & 1) * 256>(af[(s) & 3][m], ap, (m) >> 1 ? aoff2 : aoff); \
        if ((m) == 3) { \
            if ((s) < 12) ap += 8192; else if ((s) == 12) ap += wv < 2 ? 8192 : 0; \
            else if ((s) == 13) ap = ab + wv * 8192; else ap += 2048; \
            SPIN_(ap); } } while (0)
    sfor<16>([&](auto I) { ld_a(I.value >> 2, I.value & 3); });
    const lds_cptr wb = (lds_cptr)lds + wv * 5120 + ((g >> 1) * 16 + (g & 1) * 4 + ((l >> 2) & 3)) * 160 + (l & 3) * 8;
    f32x4 c2[4][5] = {};
    bf16x8 w2[2][5];
#pragma unroll
    for (int n = 0; n < 5; ++n) w2[0][n] = cat(ldstr(wb + n * 32), ldstr(wb + 8 * 160 + n * 32));
    const int orow = rb2 * 64 + wv * 16 + (l & 15), ocol = cb2 * 80 + g * 4;
    u32x2 res[5];
    sfor<19>([&](auto S_) {
        constexpr int s = S_.value;
        if constexpr (s == 14) {
            LGKM0();
            sfor<5>([&](auto Q) { dma_d(18, Q.value); });
            sfor<5>([&](auto N) { gld8<N.value * 32>(res[N.value], x, (unsigned)(orow * DM + ocol) * 2); });
            STAMP(6);
            await(ws, (unsigned)WS_FAT + ((rb2 >> 2) * 32 + (l & 31)) * 8, (unsigned)WS_FAT + ((rb2 >> 2) * 32 + (l & 31)) * 8, tag);
            STAMP(7);
            sfor<16>([&](auto I) { ld_a(14 + (I.value >> 2), I.value & 3); });
            const unsigned eo = (unsigned)(32768 + wv * 2048 + ((g >> 1) * 4 + (g & 1)) * 256 + (l & 15) * 8);
            sfor<8>([&](auto I) { constexpr int m = I.value >> 1, q = I.value & 1;
                gld8c<q * 512 + (m & 1) * 128>(ae[m][q], ab, eo + (m >> 1) * NUNIT * 1024); });
        }
        constexpr int WAITS[19] = {12, 17, 22, 18, 18, 18, 18, 18, 18, 18, 18, 14, 10, 10, 20, 16, 12, 8, 0};
        SBAR();
        LGKM0(); wait_vm<WAITS[s]>();
        if constexpr (s < 18) sfor<4>([&](auto M) { pinv(af[s & 3][M.value]); });
        else { sfor<8>([&](auto M) { pinv(ae[M.value >> 1][M.value & 1]); }); sfor<5>([&](auto M) { pinv(res[M.value]); }); }
        if (s == 13 && wv >= 2) {
#pragma unroll
            for (int m = 0; m < 4; ++m) af[1][m] = u32x4{0, 0, 0, 0};
        }
        sfor<4>([&](auto M) {
            constexpr int m = M.value;
            const bf16x8 a = s < 18 ? __builtin_bit_cast(bf16x8, af[s & 3][m])
                                    : __builtin_bit_cast(bf16x8, u32x4{ae[m][0][0], ae[m][0][1], ae[m][1][0], ae[m][1][1]});
            sfor<5>([&](auto N) {
                constexpr int n = N.value;
                SBAR();
                c2[m][n] = MFMA16(w2[s & 1][n], a, c2[m][n]);
                SBAR();
                if (m == 0 && s < 18) {
                    const lds_cptr p = wb + ((s + 1) & 3) * 20480 + n * 32;
                    w2[(s + 1) & 1][n] = cat(ldstr(p), ldstr(p + 8 * 160));
                }
                if constexpr (s < 14 && (((m == 1 || m == 2) && (n & 1)) || (m == 3 && n == 1))) dma_d(s + 4, (m - 1) * 2 + (n >> 1));
                if constexpr (s < 10 && n == 4) ld_a(s + 4, m);
            });
        });
        SBAR();
    });
    {
        const lds_ptr rp = (lds_ptr)lds + wv * 5120 + l * 16;
#pragma unroll
        for (int m = 0; m < 4; ++m)
#pragma unroll
            for (int n = 0; n < 5; ++n) *(__attribute__((address_space(3))) f32x4*)(rp + m * 20480 + n * 1024) = c2[m][n];
        asm volatile("s_waitcnt lgkmcnt(0)\n\ts_barrier" ::: "memory");
        const lds_cptr rq = (lds_cptr)lds + wv * 20480 + l * 16;
#pragma unroll
        for (int n = 0; n < 5; ++n) {
            f32x4 s = *(const __attribute__((address_space(3))) f32x4*)(rq + n * 1024);
#pragma unroll
            for (int p = 1; p < 4; ++p) s += *(const __attribute__((address_space(3))) f32x4*)(rq + p * 5120 + n * 1024);
            s[0] += __uint_as_float(res[n][0] << 16); s[1] += __uint_as_float(res[n][0] & 0xffff0000u);
            s[2] += __uint_as_float(res[n][1] << 16); s[3] += __uint_as_float(res[n][1] & 0xffff0000u);
            *(u32x2*)(out + (size_t)orow * DM + ocol + n * 16) = u32x2{cvtpk(s[0], s[1]), cvtpk(s[2], s[3])};
        }
    }
    STAMP(8);
}

__global__ __launch_bounds__(256, 2) void fused_block(const __hip_bfloat16* __restrict__ x, const __hip_bfloat16* __restrict__ wqkv,
                                                      const __hip_bfloat16* __restrict__ wo, const __hip_bfloat16* __restrict__ wg,
                                                      const __hip_bfloat16* __restrict__ wu, const __hip_bfloat16* __restrict__ wd,
                                                      char* ws, __hip_bfloat16* __restrict__ out) {
    __shared__ __attribute__((aligned(16))) char lds[LDS_BYTES];
    u64 epoch = *(const __attribute__((address_space(4))) u64*)(ws + WS_EPOCH);
    const int b = blockIdx.x;
    STAMP(0);
    if (b >= 256) { role_b(x, wo, wg, wu, wd, ws, out, lds, b - 256, epoch); return; }
    __builtin_amdgcn_s_setprio(PRIO_A);
    const int xcd = b & 7, i = b >> 3;
    int flag = -1;
    if (i < 30) {
        const int li = (xcd & 1) * 30 + i, rb = (xcd >> 1) * 4 + (li & 3), cb = li >> 2;
        if (cb >= 10) qkv_block<true>(x, wqkv, ws, lds, rb, cb, epoch); else qkv_block<false>(x, wqkv, ws, lds, rb, cb, epoch);
        flag = cb * 16 + rb;
    } else if (b < 255) {
        colsum_block(wqkv, ws, lds, b - 240, epoch);
    }
    asm volatile("" : "+s"(epoch));
    const u64 tag = epoch * 0x9E3779B97F4A7C15ull + 1;
    if (flag >= 0) publish(ws, WS_FQKV + flag * 8, tag);
    STAMP(3);
    attn_block(ws, lds, xcd * 4 + (i & 3), i >> 2, tag);
    if (b == 0 && threadIdx.x < 64) {
#pragma nounroll
        for (unsigned spin = 0; spin < SPIN_CAP; ++spin) {
            bool ok = true;
#pragma unroll
            for (int q = 0; q < 4; ++q)
                ok = ok & (__hip_atomic_load((gu64*)(ws + WS_FAT) + q * 64 + threadIdx.x, RLX_AGENT) == tag)
                        & (__hip_atomic_load((gu64*)(ws + WS_FLN) + q * 64 + threadIdx.x, RLX_AGENT) == tag);
            if (__all(ok)) break;
            __builtin_amdgcn_s_sleep(2);
        }
        if (threadIdx.x == 0) __hip_atomic_store((gu64*)(ws + WS_EPOCH), tag, RLX_AGENT);
    }
    STAMP(7);
}

extern "C" void kernel_launch(void* const* d_in, const int* in_sizes, int n_in, void* d_out, int out_size, void* d_ws, size_t ws_size,
                              hipStream_t stream) {
    fused_block<<<512, 256, 0, stream>>>((const __hip_bfloat16*)d_in[0], (const __hip_bfloat16*)d_in[1], (const __hip_bfloat16*)d_in[2],
                                         (const __hip_bfloat16*)d_in[4], (const __hip_bfloat16*)d_in[5], (const __hip_bfloat16*)d_in[6],
                                         (char*)d_ws, (__hip_bfloat16*)d_out);
}
```

```cpp
#define PFC 1
#include <hip/hip_runtime.h>
#include <hip/hip_bf16.h>
#include <stdint.h>
#include <type_traits>

typedef __attribute__((ext_vector_type(8))) short bf16x8;
typedef __attribute__((ext_vector_type(4))) short s16x4;
typedef __attribute__((ext_vector_type(2))) __bf16 bf16x2;
typedef __attribute__((ext_vector_type(16))) float f32x16;
typedef __attribute__((ext_vector_type(4))) float f32x4;
typedef __attribute__((ext_vector_type(2))) float f32x2;
typedef __attribute__((ext_vector_type(4))) unsigned u32x4;
typedef __attribute__((ext_vector_type(2))) unsigned u32x2;
typedef unsigned long long u64;
typedef __attribute__((address_space(3))) const char* lds_cptr;
typedef __attribute__((address_space(3))) char* lds_ptr;
typedef __attribute__((address_space(1))) u64 gu64;
typedef __attribute__((address_space(1))) unsigned gu32;

constexpr int SEQ = 2048, DM = 640, NH = 32, FF = 1728;
constexpr int NUNIT = 148;
constexpr size_t WS_EPOCH = 0;
constexpr size_t WS_ST = 16384;
constexpr size_t WS_CS = WS_ST + 32768;
constexpr size_t WS_Q = 65536;
constexpr size_t WS_KV = WS_Q + (size_t)NH * 64 * 1280;
constexpr size_t WS_XN = WS_KV + (size_t)NH * 64 * 2560;
constexpr size_t WS_AF = WS_XN + (size_t)64 * 40 * 1024;
constexpr size_t WS_WG = WS_AF + (size_t)64 * NUNIT * 1024;
constexpr size_t WS_WD = WS_WG + (size_t)16 * 280 * 1024;
constexpr size_t WS_FL = WS_WD + (size_t)74 * 40 * 1024;
#define PFQ 0
#define PFG 0
#define NREP 8
constexpr int FLN = 0, FQKV = 1, FGU = 2, FAT = 3, FGO = 4, FWG = 5, FAD = 6;
__device__ __forceinline__ unsigned flag_at(int arr, int copy, int i) { return (unsigned)WS_FL + ((arr * NREP + copy) * 16 + (i >> 4)) * 4352 + (i & 15) * 8; }
#define PRIO_Q 1
#define PRIO_QE 1
#define PRIO_A 1
constexpr int LDS_BYTES = 81920;

#define SBAR() __builtin_amdgcn_sched_barrier(0)
#define PIN(x) asm volatile("" : "+v"(x))
#define MFMA32(a, b, c) __builtin_amdgcn_mfma_f32_32x32x16_bf16(a, b, c, 0, 0, 0)
#define MFMA32K8(a, b, c) __builtin_amdgcn_mfma_f32_32x32x8bf16_1k(a, b, c, 0, 0, 0)
#define MFMA16(a, b, c) __builtin_amdgcn_mfma_f32_16x16x32_bf16(a, b, c, 0, 0, 0)
#define STR_(x) #x
#define WAIT_BAR(N) asm volatile("s_waitcnt vmcnt(" STR_(N) ") lgkmcnt(0)\n\ts_barrier" ::: "memory")
#define WAIT_VM(N) asm volatile("s_waitcnt vmcnt(" STR_(N) ")" ::: "memory")
#define LGKM0() __builtin_amdgcn_s_waitcnt(0xc07f)
#define RLX_AGENT __ATOMIC_RELAXED, __HIP_MEMORY_SCOPE_AGENT
template <int N> __device__ __forceinline__ void wait_bar() {
    LGKM0(); asm volatile("s_waitcnt vmcnt(%0)\n\ts_barrier" ::"n"(N) : "memory"); }
template <int N> __device__ __forceinline__ void wait_vm() { asm volatile("s_waitcnt vmcnt(%0)" ::"n"(N) : "memory"); }

__device__ __forceinline__ unsigned cvtpk(float lo, float hi) {
    return __builtin_bit_cast(unsigned, __builtin_convertvector((f32x2{lo, hi}), bf16x2)); }
template <int LO> __device__ __forceinline__ void glds16(const void* sbase, unsigned voff, unsigned lds_base) {
    asm volatile("s_add_u32 m0, %2, %3\n\ts_nop 0\n\tglobal_load_lds_dwordx4 %0, %1" ::"v"(voff), "s"(sbase), "s"(lds_base), "n"(LO) : "memory", "scc"); }
template <int O> __device__ __forceinline__ void glds16c(const void* sbase, unsigned voff, unsigned lds_base) {
    asm volatile("s_mov_b32 m0, %2\n\ts_nop 0\n\tglobal_load_lds_dwordx4 %0, %1 offset:%3 sc1" ::"v"(voff), "s"(sbase), "s"(lds_base), "n"(O) : "memory"); }
template <int LO> __device__ __forceinline__ void set_m0(unsigned lds_base) {
    asm volatile("s_add_u32 m0, %0, %1" ::"s"(lds_base), "n"(LO) : "memory", "scc"); }
template <int O, bool HALF = false> __device__ __forceinline__ void glds16cm(const void* sbase, unsigned voff) {
    if (HALF) asm volatile("s_mov_b32 exec_hi, 0\n\tglobal_load_lds_dwordx4 %0, %1 offset:%2 sc1\n\ts_mov_b32 exec_hi, -1" ::"v"(voff), "s"(sbase), "n"(O) : "memory");
    else asm volatile("global_load_lds_dwordx4 %0, %1 offset:%2 sc1" ::"v"(voff), "s"(sbase), "n"(O) : "memory"); }
template <int O = 0> __device__ __forceinline__ void gld16c(u32x4& d, const void* sbase, unsigned voff) {
    asm volatile("global_load_dwordx4 %0, %1, %2 offset:%3 sc1" : "=v"(d) : "v"(voff), "s"(sbase), "n"(O) : "memory"); }
template <int O = 0> __device__ __forceinline__ void gld8c(u32x2& d, const void* sbase, unsigned voff) {
    asm volatile("global_load_dwordx2 %0, %1, %2 offset:%3 sc1" : "=v"(d) : "v"(voff), "s"(sbase), "n"(O) : "memory"); }
__device__ __forceinline__ void touch(unsigned& d, const void* sbase, unsigned voff) {
    asm volatile("global_load_dword %0, %1, %2 sc1" : "+v"(d) : "v"(voff), "s"(sbase) : "memory"); }
template <class T> __device__ __forceinline__ void SPIN_(T& p) { asm volatile("" : "+s"(p)); }
template <class T> __device__ __forceinline__ void pinv(T& p) { asm volatile("" : "+v"(p)); }
template <class T> __device__ __forceinline__ void keep(const T& p) { asm volatile("" ::"v"(p)); }
template <int N, class F> __device__ __forceinline__ void sfor(F&& f) {
    if constexpr (N > 0) { sfor<N - 1>(f); f(std::integral_constant<int, N - 1>{}); } }
__device__ __forceinline__ void gst16c(void* sbase, unsigned voff, u32x4 v) {
    asm volatile("global_store_dwordx4 %0, %1, %2 sc1\n\ts_nop 1" ::"v"(voff), "v"(v), "s"(sbase) : "memory"); }
__device__ __forceinline__ void gst8c(void* sbase, unsigned voff, u32x2 v) {
    asm volatile("global_store_dwordx2 %0, %1, %2 sc1" ::"v"(voff), "v"(v), "s"(sbase) : "memory"); }
__device__ __forceinline__ bf16x8 ldsr128(lds_cptr p) { return *(const __attribute__((address_space(3))) bf16x8*)p; }
__device__ __forceinline__ s16x4 ldsr64(lds_cptr p) { return *(const __attribute__((address_space(3))) s16x4*)p; }
__device__ __forceinline__ s16x4 ldstr(lds_cptr p) {
    return __builtin_amdgcn_ds_read_tr16_b64_v4i16((__attribute__((address_space(3))) s16x4*)p); }
__device__ __forceinline__ bf16x8 cat(s16x4 a, s16x4 b) { return __builtin_shufflevector(a, b, 0, 1, 2, 3, 4, 5, 6, 7); }
__device__ __forceinline__ float dot2(unsigned a, unsigned b, float c) {
    return __builtin_amdgcn_fdot2_f32_bf16(__builtin_bit_cast(bf16x2, a), __builtin_bit_cast(bf16x2, b), c, false); }
__device__ __forceinline__ float xhalf(float v) { return __shfl_xor(v, 32); }
#define STAMP(slot)
constexpr unsigned ONES2 = 0x3f803f80u, EMPTY = 0xffffffffu;
constexpr float LN_EPS = 1e-5f;
constexpr unsigned SPIN_CAP = 1u << 20;

template <int R = NREP> __device__ __forceinline__ void publish(char* ws, int arr, int i, u64 tag) {
    WAIT_BAR(0);
    if (threadIdx.x < R) __hip_atomic_store((gu64*)(ws + flag_at(arr, threadIdx.x, i)), tag, RLX_AGENT);
}
#define POLLS 4
template <int NT, int R = NREP, int NAP = POLLS> __device__ __forceinline__ void await(char* ws, int arr, int i, u64 tag) {
    if (NT == 256 || threadIdx.x < NT) {
        const gu64* f = (const gu64*)(ws + flag_at(arr, (blockIdx.x >> 3) % R, i));
        bool ok = false;
#pragma nounroll
        for (unsigned spin = 0; spin < SPIN_CAP; ++spin) {
            if (!ok) ok = __hip_atomic_load(f, RLX_AGENT) == tag;
            if (__all(ok)) break;
            __builtin_amdgcn_s_sleep(NAP);
        }
    }
    asm volatile("s_barrier" ::: "memory");
}

constexpr int QX = 0, QW = 49152;

template <bool VT>
__device__ __forceinline__ void qkv_block(const __hip_bfloat16* __restrict__ x, const __hip_bfloat16* __restrict__ w,
                                          char* __restrict__ ws, char* lds, int rb, int cb, u64 epoch) {
    const int tid = threadIdx.x, l = tid & 63, wv = __builtin_amdgcn_readfirstlane(tid >> 6);
    const int wm = wv >> 1, wn = wv & 1, h = l >> 5, r32 = l & 31;
    const unsigned lds0 = (unsigned)(uintptr_t)lds;
    unsigned xo[4], wo_[2];
#pragma unroll
    for (int i = 0; i < 4; ++i) {
        const int r = 32 * wv + 8 * i + (l >> 3);
        xo[i] = (unsigned)(r * DM + (((l & 7) ^ ((r >> 1) & 7)) * 8)) * 2;
    }
#pragma unroll
    for (int i = 0; i < 2; ++i) {
        const int kr = 8 * wv + 4 * i + (l >> 4);
        wo_[i] = (unsigned)(kr * (3 * DM) + (((l & 15) ^ ((kr & 3) << 2)) * 8)) * 2;
    }
    const char* xb = (const char*)(x + (size_t)rb * 128 * DM);
    const char* wb = (const char*)(w + cb * 128);
    const unsigned lx = lds0 + QX + wv * 4096, lw = lds0 + QW + wv * 2048;
#define dma_x(kt, i) do { glds16<((kt) % 3) * 16384 + (i) * 1024>(xb, xo[i], lx); if ((i) == 3) { xb += 128; SPIN_(xb); } } while (0)
#define dma_w(hs, i) do { glds16<((hs) & 3) * 8192 + (i) * 1024>(wb, wo_[i], lw); if ((i) == 1) { wb += 32 * 3 * DM * 2; SPIN_(wb); } } while (0)
    dma_x(0, 0); dma_x(0, 1); dma_x(0, 2); dma_x(0, 3); dma_w(0, 0); dma_w(0, 1);
    dma_w(1, 0); dma_w(1, 1); dma_x(1, 0); dma_x(1, 1);
    dma_w(2, 0); dma_w(2, 1); dma_x(1, 2); dma_x(1, 3);
    unsigned pf = 0;
#pragma unroll
    for (int i = 0; i < 2; ++i) {
        const int n = wv * 2 + i;
        const bool isx = cb != 7 && n < 3;
        const int ix = min(((cb & 7) * 3 + n) * 49 + min(l, 48), 1023);
        const int iw = cb == 7 ? 64 + (n * 2 + (rb & 1)) * 64 + l : min(((n - 3) * 4 + (rb & 3)) * 55 + min(l, 54), 1087);
        if (PFQ) touch(pf, isx ? xb : wb, isx ? (ix & 127) * DM * 2 + (ix >> 7) * 128 : (iw >> 1) * 3 * DM * 2 + (iw & 1) * 128);
    }
    int xa[4], wa[2];
#pragma unroll
    for (int ks = 0; ks < 4; ++ks) xa[ks] = QX + (wm * 64 + r32) * 128 + (((2 * ks + h) ^ ((r32 >> 1) & 7)) * 16);
    {
        const int gi = l >> 4, p = l & 3, q = (l >> 2) & 3;
#pragma unroll
        for (int nt = 0; nt < 2; ++nt) {
            const int ch = wn * 8 + nt * 4 + (gi & 1) * 2 + (p >> 1);
            wa[nt] = QW + ((gi >> 1) * 8 + q) * 256 + ((ch ^ (q << 2)) * 16) + (p & 1) * 8;
        }
    }
    const unsigned so = (unsigned)(WS_ST + (rb * 128 + wm * 64 + (VT ? l : r32)) * 16), co = (unsigned)(WS_CS + (cb * 128 + wn * 64 + (VT ? r32 : l)) * 8);
    u32x4 gs[2]; u32x2 gc[2];
    f32x4 tb[VT ? 16 : 8];
    auto request = [&]() {
        gld16c(gs[0], ws, so); gld8c(gc[0], ws, co);
        if (VT) gld8c<256>(gc[1], ws, co); else gld16c<512>(gs[1], ws, so); };
    f32x16 acc[2][2] = {};
    bf16x8 xf[2][2], wf[2][2];
    const bool isq = cb < 5;
    const int tstr = isq ? 1280 : 2560;
    unsigned eo[4], es[4], trade = 0;
    if (!VT) {
        const int g0 = (isq ? cb : cb - 5) * 16 + wn * 8;
#pragma unroll
        for (int p = 0; p < 4; ++p) {
            const int G = g0 + 2 * p + h, P = (G * 205) >> 10, r = G - 5 * P;
            const int head = 2 * P + (r > 2 || (r == 2 && (l & 1)));
            const int o = r == 2 ? 1024 + (r32 >> 1) * 16 : (r > 2 ? r - 3 : r) * 512 + r32 * 16;
            eo[p] = (unsigned)((head * 64 + rb * 4 + wm * 2) * tstr + o);
            es[p] = r == 2 ? 2 - (l & 1) : 0;
            const int r0 = (g0 + 2 * p) % 5;
            trade |= (r0 == 1 || r0 == 2) << p;
            pinv(eo[p]); pinv(es[p]);
        }
        trade = __builtin_amdgcn_readfirstlane(trade); SPIN_(trade);
    } else {
#pragma unroll
        for (int nt = 0; nt < 2; ++nt) {
            const int f = (cb - 10) * 128 + wn * 64 + nt * 32 + r32, head = (f * 3277) >> 16, d = f - head * 20;
            eo[nt] = (unsigned)((head * 64 + rb * 4 + wm * 2) * 2560 + 1280 + (h * 20 + d) * 16);
            pinv(eo[nt]);
        }
    }
    sfor<41>([&](auto S_) {
        constexpr int s = S_.value, hs = s >> 1, kt = s >> 2, cur = s & 1, prv = cur ^ 1;
        if (s < 40 && cur == 0) {
            if (hs < 3 && PFQ) wait_bar<10>(); else if (hs < 17) wait_bar<8>(); else if (hs == 17) wait_bar<9>(); else if (hs == 18) wait_bar<5>(); else wait_bar<0>();
            if (hs == 0) STAMP(1);
            if (hs == 3) pinv(pf);
        } else LGKM0();
        SBAR();
        if (s > 0) acc[0][0] = VT ? MFMA32(xf[prv][0], wf[prv][0], acc[0][0]) : MFMA32(wf[prv][0], xf[prv][0], acc[0][0]);
        SBAR();
        if (s < 40) {
            const lds_cptr xs = (lds_cptr)lds + (kt % 3) * 16384 + xa[s & 3];
            const lds_cptr wp = (lds_cptr)lds + (hs & 3) * 8192 + cur * 4096;
            xf[cur][0] = ldsr128(xs);
            wf[cur][0] = cat(ldstr(wp + wa[0]), ldstr(wp + wa[0] + 1024));
            wf[cur][1] = cat(ldstr(wp + wa[1]), ldstr(wp + wa[1] + 1024));
            xf[cur][1] = ldsr128(xs + 4096);
        }
        SBAR();
        if (s > 0) acc[0][1] = VT ? MFMA32(xf[prv][0], wf[prv][1], acc[0][1]) : MFMA32(wf[prv][1], xf[prv][0], acc[0][1]);
        SBAR();
        if (s == 32) request();
        if (s == 38) {
            SPIN_(epoch);
            const unsigned tag = (unsigned)(epoch * 0x9E3779B97F4A7C15ull + 1);
#pragma nounroll
            for (unsigned spin = 0; spin < SPIN_CAP; ++spin) {
                pinv(gs[0]); pinv(gc[0]); if (VT) pinv(gc[1]); else pinv(gs[1]);
                const bool ok = gs[0][1] == tag && gs[0][3] == tag && gc[0][1] == tag && (VT ? gc[1][1] == tag : gs[1][1] == tag && gs[1][3] == tag);
                if (__builtin_expect(__all(ok), 1)) break;
                request(); wait_vm<0>();
            }
            const lds_ptr tp = (lds_ptr)lds + QW + wv * 512;
            if (VT) *(__attribute__((address_space(3))) u32x2*)(tp + l * 8) = u32x2{gs[0][0], gs[0][2]};
            else *(__attribute__((address_space(3))) unsigned*)(tp + l * 4) = gc[0][0];
        }
        if (s == 39) {
            const lds_cptr tp = (lds_cptr)lds + QW + wv * 512;
#pragma unroll
            for (int i = 0; i < (VT ? 16 : 8); ++i)
                tb[i] = *(const __attribute__((address_space(3))) f32x4*)(tp + (VT ? (i >> 1) * 64 + h * 32 + (i & 1) * 16 : i * 32 + h * 16));
        }
        if (cur == 0) { if (hs + 3 < 20) dma_w(hs + 3, 0); } else if (kt + 2 < 10) dma_x(kt + 2, (hs & 1) * 2);
        SBAR();
        if (s > 0) acc[1][0] = VT ? MFMA32(xf[prv][1], wf[prv][0], acc[1][0]) : MFMA32(wf[prv][0], xf[prv][1], acc[1][0]);
        SBAR();
        if (cur == 0) { if (hs + 3 < 20) dma_w(hs + 3, 1); } else if (kt + 2 < 10) dma_x(kt + 2, (hs & 1) * 2 + 1);
        SBAR();
        if (s > 0) acc[1][1] = VT ? MFMA32(xf[prv][1], wf[prv][1], acc[1][1]) : MFMA32(wf[prv][1], xf[prv][1], acc[1][1]);
        SBAR();
    });
    STAMP(2);
    __builtin_amdgcn_s_setprio(PRIO_QE);
    if (!VT) {
        const float osc = isq ? 0.22360679774997896f * 1.4426950408889634f : 1.f;
        char* ob = ws + (isq ? WS_Q : WS_KV);
#pragma unroll
        for (int p = 0; p < 4; ++p) {
            const unsigned oo = eo[p];
#pragma unroll
            for (int mt = 0; mt < 2; ++mt) {
                const float mu = __uint_as_float(gs[mt][0]), rs = __uint_as_float(gs[mt][2]) * osc;
                unsigned q[2][2];
#pragma unroll
                for (int e = 0; e < 2; ++e) {
                    const int g = 2 * p + e;
                    float v[4];
#pragma unroll
                    for (int j = 0; j < 4; ++j) v[j] = (acc[mt][g >> 2][(g & 3) * 4 + j] - mu * tb[g][j]) * rs;
                    q[e][0] = cvtpk(v[0], v[1]); q[e][1] = cvtpk(v[2], v[3]);
                }
                const auto s0 = __builtin_amdgcn_permlane32_swap(q[0][0], q[1][0], false, false);
                const auto s1 = __builtin_amdgcn_permlane32_swap(q[0][1], q[1][1], false, false);
                u32x4 v = {s0[0], s1[0], s0[1], s1[1]};
                if (trade >> p & 1) {
                    const bool odd = l & 1;
                    const unsigned t0 = __builtin_amdgcn_mov_dpp(odd ? v[0] : v[2], 0xb1, 0xf, 0xf, true);
                    const unsigned t1 = __builtin_amdgcn_mov_dpp(odd ? v[1] : v[3], 0xb1, 0xf, 0xf, true);
                    const bool lo = es[p] == 1, hi = es[p] == 2;
                    v[0] = lo ? t0 : v[0]; v[1] = lo ? t1 : v[1]; v[2] = hi ? t0 : v[2]; v[3] = hi ? t1 : v[3];
                }
                gst16c(ob + mt * tstr, oo, v);
            }
        }
    } else {
#pragma unroll
        for (int nt = 0; nt < 2; ++nt) {
            const float c = __uint_as_float(gc[nt][0]);
            const unsigned oo = eo[nt];
#pragma unroll
            for (int mt = 0; mt < 2; ++mt)
#pragma unroll
                for (int s = 0; s < 2; ++s) {
                    float v[8];
#pragma unroll
                    for (int j = 0; j < 8; ++j) {
                        const f32x4 mr = tb[mt * 8 + 4 * s + (j >> 1)];
                        v[j] = (acc[mt][nt][8 * s + j] - mr[(j & 1) * 2] * c) * mr[(j & 1) * 2 + 1];
                    }
                    gst16c(ws + WS_KV + mt * 2560 + s * 640, oo, u32x4{cvtpk(v[0], v[1]), cvtpk(v[2], v[3]), cvtpk(v[4], v[5]), cvtpk(v[6], v[7])});
                }
        }
    }
}

__device__ __forceinline__ void colsum_block(const __hip_bfloat16* __restrict__ w, char* __restrict__ ws, char* lds, int cb, u64 epoch) {
    const int tid = threadIdx.x, c = tid & 15, rg = tid >> 4;
    const u32x4* src = (const u32x4*)(w + (size_t)rg * 3 * DM + cb * 128 + c * 8);
    u32x4 v[40];
#pragma unroll
    for (int i = 0; i < 40; ++i) v[i] = src[(size_t)i * 16 * 3 * DM / 8];
    float s[8] = {};
    unsigned pick_lo = 0x00003f80u, pick_hi = 0x3f800000u;
    pinv(pick_lo); pinv(pick_hi);
#pragma unroll
    for (int i = 0; i < 40; ++i)
#pragma unroll
        for (int j = 0; j < 4; ++j) { s[2 * j] = dot2(v[i][j], pick_lo, s[2 * j]); s[2 * j + 1] = dot2(v[i][j], pick_hi, s[2 * j + 1]); }
    float* red = (float*)lds;
    *(f32x4*)(red + rg * 128 + c * 8) = f32x4{s[0], s[1], s[2], s[3]};
    *(f32x4*)(red + rg * 128 + c * 8 + 4) = f32x4{s[4], s[5], s[6], s[7]};
    __syncthreads();
    if (tid < 128) {
        float t = 0.f;
#pragma unroll
        for (int i = 0; i < 16; ++i) t += red[i * 128 + tid];
        gst8c(ws, (unsigned)(WS_CS + (cb * 128 + tid) * 8), u32x2{__float_as_uint(t), (unsigned)(epoch * 0x9E3779B97F4A7C15ull + 1)});
    }
    __syncthreads();
}

constexpr int AT_PAIR = 5152, AT_CHUNK = 4 * AT_PAIR;
constexpr float SHIFT_MARGIN = 64.f;

#define EXP4(P, S, o) do { \
    float e0 = __builtin_amdgcn_exp2f(S[o + 0]), e1 = __builtin_amdgcn_exp2f(S[o + 1]); \
    float e2 = __builtin_amdgcn_exp2f(S[o + 2]), e3 = __builtin_amdgcn_exp2f(S[o + 3]); \
    P[((o) >> 1) & 3] = cvtpk(e0, e1); P[(((o) >> 1) & 3) + 1] = cvtpk(e2, e3); } while (0)
#define GRP(S, o, P, j) do { \
    const float n0 = __builtin_amdgcn_exp2f(S[o + 0]); SBAR(); P[j] = cvtpk(e0, e1); SBAR(); \
    const float n1 = __builtin_amdgcn_exp2f(S[o + 1]); SBAR(); P[j + 1] = cvtpk(e2, e3); SBAR(); \
    e0 = n0; e1 = n1; e2 = __builtin_amdgcn_exp2f(S[o + 2]); SBAR(); e3 = __builtin_amdgcn_exp2f(S[o + 3]); } while (0)

__device__ __forceinline__ float max16(const f32x16& s) {
    float a = fmaxf(fmaxf(s[0], s[1]), s[2]), b = fmaxf(fmaxf(s[3], s[4]), s[5]);
    a = fmaxf(fmaxf(a, s[6]), s[7]); b = fmaxf(fmaxf(b, s[8]), s[9]);
    a = fmaxf(fmaxf(a, s[10]), s[11]); b = fmaxf(fmaxf(b, s[12]), s[13]);
    return fmaxf(fmaxf(a, s[14]), fmaxf(b, s[15]));
}
__device__ __forceinline__ f32x16 splat16(float v) { f32x16 r;
#pragma unroll
    for (int i = 0; i < 16; ++i) r[i] = v;
    return r; }

__device__ __forceinline__ f32x16 attn_fallback(const char* kv, bf16x8 q1, s16x4 q2, int l) {
    const int h = l >> 5, r32 = l & 31, d = r32 < 20 ? r32 : 0;
    float m = -INFINITY; f32x16 o = {};
    const f32x16 z = {};
    for (int t = 0; t < 64; ++t) {
        u32x4 k1, v0, v1; u32x2 k2;
        gld16c(k1, kv, t * 2560 + l * 16); gld8c(k2, kv, t * 2560 + 1024 + r32 * 8);
        gld16c(v0, kv, t * 2560 + 1280 + (h * 20 + d) * 16); gld16c(v1, kv, t * 2560 + 1280 + ((2 + h) * 20 + d) * 16);
        asm volatile("s_waitcnt vmcnt(0)" : "+v"(k1), "+v"(k2), "+v"(v0), "+v"(v1)::"memory");
        f32x16 s = MFMA32(__builtin_bit_cast(bf16x8, k1), q1, z); s = MFMA32K8(__builtin_bit_cast(s16x4, k2), q2, s);
        float tm = max16(s); tm = fmaxf(tm, xhalf(tm));
        const float mn = fmaxf(m, tm), al = __builtin_amdgcn_exp2f(m - mn); m = mn;
        u32x4 p0, p1;
#pragma unroll
        for (int i = 0; i < 16; ++i) { s[i] -= mn; o[i] *= al; }
        EXP4(p0, s, 0); EXP4(p0, s, 4); EXP4(p1, s, 8); EXP4(p1, s, 12);
        const u32x4 ones = {ONES2, ONES2, ONES2, ONES2};
        if (r32 >= 20) { v0 = ones; v1 = ones; }
        o = MFMA32(__builtin_bit_cast(bf16x8, v0), __builtin_bit_cast(bf16x8, p0), o);
        o = MFMA32(__builtin_bit_cast(bf16x8, v1), __builtin_bit_cast(bf16x8, p1), o);
    }
    return o;
}

__device__ __forceinline__ bool attn_bad(const f32x16& o) {
    const float a = (fabsf(o[0]) + fabsf(o[1])) + (fabsf(o[2]) + fabsf(o[3])), b = (fabsf(o[4]) + fabsf(o[5])) + (fabsf(o[6]) + fabsf(o[7]));
    const float c = (fabsf(o[8]) + fabsf(o[9])) + (fabsf(o[10]) + fabsf(o[11]));
    return __builtin_amdgcn_ballot_w64(!(a + b + c < 1e37f)) != 0;
}
__device__ __forceinline__ void attn_finish(const f32x16& o, char* ws, int head, int rt, int l) {
    const int h = l >> 5, r32 = l & 31;
    const unsigned lb = __float_as_uint(o[8]);
    const float inv = __builtin_amdgcn_rcpf(__uint_as_float(__builtin_amdgcn_permlane32_swap(lb, lb, false, false)[1]));
    char* dst = ws + WS_AF + (size_t)rt * NUNIT * 1024;
#define OPK(i) min(cvtpk(o[i] * inv, o[i + 1] * inv), EMPTY - 1)
    gst16c(dst, head * 1024 + l * 16, u32x4{OPK(0), OPK(2), OPK(4), OPK(6)});
    if (h == 0) gst8c(dst, 32768 + head * 256 + r32 * 8, u32x2{OPK(8), OPK(10)});
}

#define HINTC 7
#define HNAP 0
#define HALL 0
__device__ __forceinline__ void attn_block(char* ws, char* lds, int head, int qb, u64 tag) {
    const int tid = threadIdx.x, l = tid & 63, wv = __builtin_amdgcn_readfirstlane(tid >> 6), h = l >> 5, r32 = l & 31;
    const unsigned lds0 = (unsigned)(uintptr_t)lds;
    const char* kv = ws + WS_KV + (size_t)head * 64 * 2560;
    const unsigned voff = l * 16;
    if (tid < 96) *(__attribute__((address_space(3))) unsigned*)((lds_ptr)lds + (tid >> 3) * AT_PAIR + 5120 + (tid & 7) * 4) = (tid & 4) ? 0u : ONES2;
    {
        const int c0 = head * 20 >> 7, c1 = (head * 20 + 19) >> 7;
        await<128>(ws, FQKV, wv == 0 ? ((l & 32 ? 10 : 5) + (l & 16 ? c1 : c0)) * 16 + (l & 15) : (l & 2 ? c1 : c0) * 16 + 2 * qb + (l & 1), tag);
    }
    STAMP(4);
#define dma_kv(c, slot, i) glds16c<((i) < 2 ? (i) : (i) - 2) * 1024>(kv + ((c) * 4 + wv) * 5120 + ((i) < 2 ? 0 : 2048), voff, \
                                                                 lds0 + (slot) * AT_CHUNK + wv * AT_PAIR + ((i) < 2 ? 0 : 2048))
    const int rt0 = qb * 8 + wv * 2;
    const char* qp = ws + WS_Q + (size_t)(head * 64 + rt0) * 1280;
    u32x4 qa1_, qb1_; u32x2 qa2_, qb2_;
    gld16c(qa1_, qp, voff); gld16c<1280>(qb1_, qp, voff); gld8c<1024>(qa2_, qp, r32 * 8); gld8c<2304>(qb2_, qp, r32 * 8);
    dma_kv(0, 0, 0); dma_kv(0, 0, 1); dma_kv(0, 0, 2); dma_kv(0, 0, 3); dma_kv(0, 0, 4); dma_kv(1, 1, 0); dma_kv(1, 1, 1);
    const int ka1 = l * 16, ka2 = 1024 + r32 * 8;
    int va[2][2];
#pragma unroll
    for (int tp = 0; tp < 2; ++tp)
#pragma unroll
        for (int s = 0; s < 2; ++s) va[tp][s] = r32 < 20 ? tp * 2560 + 1280 + ((s * 2 + h) * 20 + r32) * 16 : (r32 == 20 ? 5120 : 5136);
    asm volatile("s_waitcnt vmcnt(2) lgkmcnt(0)\n\ts_barrier" : "+v"(qa1_), "+v"(qb1_), "+v"(qa2_), "+v"(qb2_)::"memory");
    if (h) { qa2_ = u32x2{0, 0}; qb2_ = u32x2{0, 0}; }
    const bf16x8 qa1 = __builtin_bit_cast(bf16x8, qa1_), qb1 = __builtin_bit_cast(bf16x8, qb1_);
    const s16x4 qa2 = __builtin_bit_cast(s16x4, qa2_), qb2 = __builtin_bit_cast(s16x4, qb2_);
    STAMP(5);
    {
        char* dst = ws + WS_AF + (size_t)rt0 * NUNIT * 1024;
        gst16c(dst, head * 1024 + l * 16, u32x4{EMPTY, EMPTY, EMPTY, EMPTY});
        gst16c(dst, (NUNIT + head) * 1024 + l * 16, u32x4{EMPTY, EMPTY, EMPTY, EMPTY});
        gst8c(dst, h * NUNIT * 1024 + 32768 + head * 256 + r32 * 8, u32x2{EMPTY, EMPTY});
    }
    f32x16 nma, nmb;
    {
        const f32x16 z = {};
        float ma = -INFINITY, mb = -INFINITY;
#pragma unroll
        for (int t = 0; t < 4; ++t) {
            const lds_cptr p = (lds_cptr)lds + (t >> 1) * AT_PAIR + (t & 1) * 2560;
            const bf16x8 k1 = ldsr128(p + ka1); const s16x4 k2 = ldsr64(p + ka2);
            f32x16 sa = MFMA32(k1, qa1, z), sb = MFMA32(k1, qb1, z);
            sa = MFMA32K8(k2, qa2, sa); sb = MFMA32K8(k2, qb2, sb);
            ma = fmaxf(ma, max16(sa)); mb = fmaxf(mb, max16(sb));
        }
        ma = fmaxf(ma, xhalf(ma)); mb = fmaxf(mb, xhalf(mb));
        nma = splat16(-(ma + SHIFT_MARGIN)); nmb = splat16(-(mb + SHIFT_MARGIN));
        PIN(nma); PIN(nmb);
    }
    __builtin_amdgcn_s_setprio(PRIO_A);
    f32x16 oa = {}, ob = {}, sa, sb;
    u32x4 pa0, pa1, pb0 = {}, pb1 = {};
    bf16x8 k1, v0 = {}, v1 = {}; s16x4 k2;
    k1 = ldsr128((lds_cptr)lds + ka1); k2 = ldsr64((lds_cptr)lds + ka2);
    sa = MFMA32(k1, qa1, nma); sa = MFMA32K8(k2, qa2, sa);
    float e0 = 0.f, e1 = 0.f, e2 = 0.f, e3 = 0.f;
    int slot = 0;
    for (int c = 0; c < 8; ++c) {
        const lds_cptr cb = (lds_cptr)lds + slot * AT_CHUNK;
        const int nslot = slot == 2 ? 0 : slot + 1, pslot = slot == 0 ? 2 : slot - 1;
        const lds_cptr nb = (lds_cptr)lds + nslot * AT_CHUNK;
        if (c == HINTC && tid < NREP) gst8c(ws, flag_at(FAT, tid, qb * 32 + head), __builtin_bit_cast(u32x2, tag));
        const int cn = (c + 1) & 7, cn2 = (c + 2) & 7;
#pragma unroll
        for (int t = 0; t < 8; ++t) {
            const lds_cptr kp = t < 7 ? cb + ((t + 1) >> 1) * AT_PAIR + ((t + 1) & 1) * 2560 : nb;
            const lds_cptr vp = cb + (t >> 1) * AT_PAIR;
            SBAR();
            sb = MFMA32(k1, qb1, nmb); SBAR();
            if (t == 7) { asm volatile("s_waitcnt vmcnt(0)\n\ts_barrier" ::: "memory"); SBAR(); }
            const bf16x8 nk1 = ldsr128(kp + ka1); const s16x4 nk2 = ldsr64(kp + ka2);
            const bf16x8 nv0 = ldsr128(vp + va[t & 1][0]), nv1 = ldsr128(vp + va[t & 1][1]);
            SBAR();
            GRP(sa, 0, pb1, 2); SBAR();
            sb = MFMA32K8(k2, qb2, sb); SBAR();
            GRP(sa, 4, pa0, 0); SBAR();
            ob = MFMA32(v0, __builtin_bit_cast(bf16x8, pb0), ob); SBAR();
            if (t == 7) dma_kv(cn2, pslot, 0); else if (t == 0) dma_kv(cn, nslot, 2); else if (t == 1) dma_kv(cn, nslot, 4);
            SBAR();
            GRP(sa, 8, pa0, 2); SBAR();
            ob = MFMA32(v1, __builtin_bit_cast(bf16x8, pb1), ob); SBAR();
            LGKM0(); SBAR();
            GRP(sa, 12, pa1, 0); SBAR();
            k1 = nk1; k2 = nk2; v0 = nv0; v1 = nv1;
            sa = MFMA32(k1, qa1, nma); SBAR();
            GRP(sb, 0, pa1, 2); SBAR();
            sa = MFMA32K8(k2, qa2, sa); SBAR();
            GRP(sb, 4, pb0, 0); SBAR();
            oa = MFMA32(v0, __builtin_bit_cast(bf16x8, pa0), oa); SBAR();
            if (t == 7) dma_kv(cn2, pslot, 1); else if (t == 0) dma_kv(cn, nslot, 3);
            SBAR();
            GRP(sb, 8, pb0, 2); SBAR();
            oa = MFMA32(v1, __builtin_bit_cast(bf16x8, pa1), oa); SBAR();
            GRP(sb, 12, pb1, 0); SBAR();
        }
        slot = nslot;
    }
    pb1[2] = cvtpk(e0, e1); pb1[3] = cvtpk(e2, e3);
    ob = MFMA32(v0, __builtin_bit_cast(bf16x8, pb0), ob);
    ob = MFMA32(v1, __builtin_bit_cast(bf16x8, pb1), ob);
    STAMP(6);
    if (__builtin_expect(attn_bad(oa), 0)) oa = attn_fallback(kv, qa1, qa2, l);
    attn_finish(oa, ws, head, rt0, l);
    if (__builtin_expect(attn_bad(ob), 0)) ob = attn_fallback(kv, qb1, qb2, l);
    attn_finish(ob, ws, head, rt0 + 1, l);
    publish<1>(ws, FAD, qb * 32 + head, tag);
}

#define GONAP 8
#define NRL 2
#define PFM 0
#define WSOFF 0
#define GOQ 0
#define PRIO_GU 1
#define PRIO_EP 2
#define PRIO_DN 1
__device__ __forceinline__ void gld16(u32x4& d, const void* sbase, unsigned voff) {
    asm volatile("global_load_dwordx4 %0, %1, %2" : "=v"(d) : "v"(voff), "s"(sbase) : "memory"); }
template <int O> __device__ __forceinline__ void gld8(u32x2& d, const void* sbase, unsigned voff) {
    asm volatile("global_load_dwordx2 %0, %1, %2 offset:%3" : "=v"(d) : "v"(voff), "s"(sbase), "n"(O) : "memory"); }
template <int LO> __device__ __forceinline__ void glds16v(const char* src, unsigned lds_base) {
    asm volatile("s_add_u32 m0, %1, %2\n\ts_nop 0\n\tglobal_load_lds_dwordx4 %0, off" ::"v"(src), "s"(lds_base), "n"(LO) : "memory", "scc"); }
__device__ __forceinline__ constexpr int gu_slot(int kt) { return (kt & 3) == 3 ? 65536 : (kt & 3) * 14336; }

constexpr int CODE_KB = 57;
__device__ __forceinline__ void role_b(const char* code, const __hip_bfloat16* __restrict__ x, const __hip_bfloat16* __restrict__ wqkv, const __hip_bfloat16* __restrict__ wo,
                                       const __hip_bfloat16* __restrict__ wg, const __hip_bfloat16* __restrict__ wu,
                                       const __hip_bfloat16* __restrict__ wd, char* ws, __hip_bfloat16* __restrict__ out,
                                       char* lds, int j, u64 epoch) {
    const int tid = threadIdx.x, l = tid & 63, wv = __builtin_amdgcn_readfirstlane(tid >> 6);
    const unsigned lds0 = (unsigned)(uintptr_t)lds;
    const int xcd = j & 7, bi = j >> 3;
    __builtin_amdgcn_s_setprio(3);
    const int rot = wv * 3 - (wv == 3);
    u32x4 xv[3];
#pragma unroll
    for (int i = 0; i < 3; ++i)
        gld16(xv[i], (const char*)x + (size_t)j * 8 * DM * 2 + (i < 2 || wv < 2 ? rot + i : rot) * 128, (l & 7) * DM * 2 + (l >> 3) * 16);
    u32x4 pfm;
    if (PFM) gld16(pfm, code, min(bi * 4 + wv, CODE_KB - 1) * 1024 + l * 16);
    const int rb = (xcd >> 2) * 8 + (bi & 7), cbk = (xcd & 3) * 4 + (bi >> 3);
    const int u0 = (cbk * 27) >> 2, rt = rb * 4 + wv;
    const int g = l >> 4, gw = wv * 16 + rb, pd = (gw - 40) * 16 + cbk;
    const char* psrc; int pstr, pdst, pfa, pfu;
    {
        const int p = l & 3, q = (l >> 2) & 3;
        if (gw < 40) {
            psrc = nullptr; pstr = 0; pdst = (int)WS_WG + (cbk * 40 + gw) * 7168;
            pfa = ((g & 1) * 16 + (g >> 1) * 8 + q) * 224 + p * 8; pfu = 4 * 224;
        } else {
            const int st = pd < 370 ? pd / 5 : 0, cq = pd % 5;
            const int row = st < 54 ? st * 32 : st < 70 ? (st - 54) * 40 : (st - 70) * 160 + 16;
            psrc = (const char*)(st < 54 ? wd : wo) + (size_t)(row + g) * DM * 2 + cq * 256 + ((l & 15) ^ (g << 2)) * 16;
            pstr = (st < 70 ? 4 : 20) * DM * 2; pdst = (int)WS_WD + (st * 40 + cq * 8) * 1024;
            pfa = ((g >> 1) * 16 + (g & 1) * 4 + q) * 256 + (((p >> 1) ^ (q << 2)) * 16) + (p & 1) * 8; pfu = 2048;
        }
    }
    const bool pskip = pd >= 270 && pd < 350;
    if (gw < 40) {
        sfor<7>([&](auto I) {
            const int c = I.value * 64 + l, r = (c * 4682) >> 16;
            glds16v<I.value * 1024>((const char*)(r < 16 ? wg : wu) + ((gw * 16 + (r & 15)) * FF * 2 + u0 * 32 + (c - r * 14) * 16), lds0 + wv * 8192); });
    } else {
        sfor<8>([&](auto I) { glds16v<I.value * 1024>(psrc, lds0 + wv * 8192); psrc += pstr; if (I.value == 3 && pskip) psrc += pstr; });
    }
    const unsigned lg = lds0 + wv * 3584, l16 = l * 16;
    const char* gp = ws + WS_WG + cbk * 286720 + wv * 3584;
#define dma_g(i) do { glds16cm<(i) * 1024, (i) == 3>(gp, l16); if ((i) == 3) { gp += 14336; SPIN_(gp); } } while (0)
    f32x16 acc[7] = {};
    bf16x8 wf[2][7] = {};
    u32x4 xr[4][2];
    xr[3][1] = u32x4{0, 0, 0, 0};
    sfor<7>([&](auto U) { pinv(acc[U.value]); pinv(wf[1][U.value]); }); pinv(xr[3][1]);
    asm volatile("" : "+s"(epoch));
    const u64 tag = epoch * 0x9E3779B97F4A7C15ull + 1;
    asm volatile("s_waitcnt vmcnt(%3)" : "+v"(xv[0]), "+v"(xv[1]), "+v"(xv[2]) : "n"(7 + PFM) : "memory");
    {
        float s1 = 0.f, s2 = 0.f;
#pragma unroll
        for (int i = 0; i < 3; ++i)
            if (i < 2 || wv < 2) {
#pragma unroll
                for (int q = 0; q < 4; ++q) { s1 = dot2(xv[i][q], ONES2, s1); s2 = dot2(xv[i][q], xv[i][q], s2); }
            }
        s1 += __uint_as_float(__builtin_amdgcn_mov_dpp(__float_as_uint(s1), 0x128, 0xf, 0xf, true));
        s2 += __uint_as_float(__builtin_amdgcn_mov_dpp(__float_as_uint(s2), 0x128, 0xf, 0xf, true));
        const lds_ptr rp = (lds_ptr)lds + 49152 + (l & 7) * 144;
        if (!(l & 8)) *(__attribute__((address_space(3))) f32x2*)(rp + (wv * 4 + (l >> 4)) * 8) = f32x2{s1, s2};
        asm volatile("s_waitcnt lgkmcnt(0)\n\ts_barrier" ::: "memory");
        f32x4 t = *(const __attribute__((address_space(3))) f32x4*)rp;
#pragma unroll
        for (int i = 1; i < 8; ++i) t += *(const __attribute__((address_space(3))) f32x4*)(rp + i * 16);
        s1 = t[0] + t[2]; s2 = t[1] + t[3];
        const float mu = s1 * (1.f / DM), rs = rsqrtf(fmaxf(s2 * (1.f / DM) - mu * mu, 0.f) + LN_EPS), nb = -mu * rs;
        if (tid < 8) gst16c(ws, (unsigned)WS_ST + (j * 8 + tid) * 16, u32x4{__float_as_uint(mu), (unsigned)tag, __float_as_uint(rs), (unsigned)tag});
        char* dst = ws + WS_XN + (size_t)(j >> 2) * 40 * 1024;
#pragma unroll
        for (int i = 0; i < 3; ++i)
            if (i < 2 || wv < 2) {
                u32x4 o;
#pragma unroll
                for (int q = 0; q < 4; ++q) {
                    const float lo = __uint_as_float(xv[i][q] << 16), hi = __uint_as_float(xv[i][q] & 0xffff0000u);
                    o[q] = cvtpk(fmaf(lo, rs, nb), fmaf(hi, rs, nb));
                }
                gst16c(dst, (unsigned)(((rot + i) * 8 + (l >> 3)) * 512 + ((j & 3) * 8 + (l & 7)) * 16), o);
            }
    }
    wait_vm<2>();
    if (PFM) pinv(pfm);
    STAMP(9);
    if (pd < 370) {
        bf16x8 f[8];
#pragma unroll
        for (int i = 0; i < 8; ++i) {
            const lds_cptr p = (lds_cptr)lds + wv * 8192 + (gw < 40 ? pfa + i * 32 : pfa ^ (i * 32));
            f[i] = cat(ldstr(p), ldstr(p + pfu));
        }
#pragma unroll
        for (int i = 0; i < 8; ++i)
            if (i < 7 || gw >= 40) gst16c(ws, pdst + l16 + i * 1024, __builtin_bit_cast(u32x4, f[i]));
    }
    WAIT_BAR(0);
    if (tid < 2 * NRL) __hip_atomic_store((gu64*)(ws + (tid < NRL ? flag_at(FLN, tid, j) : flag_at(FWG, tid - NRL, cbk * 16 + rb))), tag, RLX_AGENT);
    STAMP(1);
    await<32, NRL, GONAP>(ws, l < 16 ? FLN : FWG, (l < 16 ? rb : cbk) * 16 + (l & 15), tag);
    STAMP(2);
    sfor<3>([&](auto K) { set_m0<gu_slot(K.value)>(lg); asm volatile("s_nop 0"); dma_g(0); dma_g(1); dma_g(2); dma_g(3); });
    __builtin_amdgcn_s_setprio(PRIO_GU);
    const char* xsrc = ws + WS_XN + (size_t)rt * 40 * 1024;
    unsigned xoff = l * 16;
#pragma unroll
    for (int kt = 0; kt < 3; ++kt) { gld16c(xr[kt][0], xsrc, xoff); gld16c<1024>(xr[kt][1], xsrc, xoff); xoff += 2048; }
    unsigned pf = 0;
    if (PFG) {
        touch(pf, ws + WS_WG + cbk * 286720 + 3 * 14336, min((wv * 8 + (bi & 7)) * 60 + min(l, 59), 1903) * 128);
        touch(pf, xsrc, 8192 + (l >> 2) * 2048 + ((bi >> 3) * 4 + (l & 3)) * 128);
    }
    const int rb2 = (xcd >> 1) * 8 + (bi & 7), cb2 = (xcd & 1) * 4 + (bi >> 3);
    const char* wp = ws + WS_WD + (wv * 40 + cb2 * 5) * 1024;
    const unsigned ld = lds0 + wv * 5120;
#define dma_d(s, q) do { \
        if ((q) == 0 || (q) == 4) { set_m0<((s) & 3) * 20480 + ((q) == 4 ? 1024 : 0)>(ld); asm volatile("s_nop 0"); } \
        if ((q) < 4) glds16cm<((q) & 3) * 1024>(wp, l16); else glds16cm<3072>(wp, l16 + 1024); \
        if ((q) == 4) { \
            if ((s) < 12) wp += 160 * 1024; else if ((s) == 12) wp += wv < 2 ? 160 * 1024 : 0; \
            else if ((s) == 13) wp = ws + WS_WD + ((54 + wv * 4) * 40 + cb2 * 5) * 1024; \
            else if ((s) < 17) wp += 40 * 1024; else if ((s) == 17) wp = ws + WS_WD + ((70 + wv) * 40 + cb2 * 5) * 1024; \
            SPIN_(wp); } } while (0)
    {
        wait_vm<PFG ? 4 : 2>();
        u32x2 go; const unsigned goff = flag_at(FGO, bi % NREP, (j >> 6) * 16);
        auto gu_step = [&](auto K4, auto LAST) {
            constexpr int k4 = K4.value; constexpr bool last = LAST.value;
            constexpr bool more = !last || k4 == 0;
            wait_bar<(last && k4 == 2) ? 6 : (last && k4 == 3) ? 0 : 12>();
            pinv(xr[k4][0]); pinv(xr[k4][1]);
            if constexpr (last && k4 == 0) gld8c(go, ws, goff);
            if constexpr (last && k4 == 3) {
                pinv(go);
                if (__builtin_expect(__any(go[0] != (unsigned)tag || go[1] != (unsigned)(tag >> 32)), 0)) {
#pragma nounroll
                    for (unsigned spin = 0; spin < SPIN_CAP && __any(__hip_atomic_load((gu64*)(ws + goff), RLX_AGENT) != tag); ++spin) __builtin_amdgcn_s_sleep(4);
                }
            }
            const lds_cptr sb = (lds_cptr)lds + gu_slot(k4) + l16;
            sfor<7>([&](auto U) {
                constexpr int u = U.value;
                SBAR();
                acc[u] = MFMA32(wf[1][u], __builtin_bit_cast(bf16x8, xr[(k4 + 3) & 3][1]), acc[u]);
                SBAR();
                wf[0][u] = ldsr128(sb + u * 1024);
                if constexpr (more && u == 0) set_m0<gu_slot(k4 + 3)>(lg);
                if constexpr (more && u >= 1 && u < 5) dma_g(u - 1);
                if constexpr (last && k4 == 3) dma_d(u / 5, u % 5);
            });
            sfor<7>([&](auto U) { keep(wf[1][U.value]); });
            sfor<7>([&](auto U) {
                constexpr int u = U.value;
                SBAR();
                acc[u] = MFMA32(wf[0][u], __builtin_bit_cast(bf16x8, xr[k4][0]), acc[u]);
                SBAR();
                if constexpr (u < 4) wf[1][2 * u] = ldsr128(sb + 7168 + 2 * u * 1024);
                if constexpr (u < 3) wf[1][2 * u + 1] = ldsr128(sb + 7168 + (2 * u + 1) * 1024);
                if (more && u == 4) gld16c(xr[(k4 + 3) & 3][0], xsrc, xoff);
                if (more && u == 5) { gld16c<1024>(xr[(k4 + 3) & 3][1], xsrc, xoff); xoff += 2048; }
                if constexpr (last && k4 == 3) dma_d((u + 7) / 5, (u + 7) % 5);
            });
            sfor<7>([&](auto U) { keep(wf[0][U.value]); });
            SBAR();
        };
        using std::integral_constant;
        for (int it = 0; it < 4; ++it) {
            gu_step(integral_constant<int, 0>{}, std::false_type{}); gu_step(integral_constant<int, 1>{}, std::false_type{});
            gu_step(integral_constant<int, 2>{}, std::false_type{}); gu_step(integral_constant<int, 3>{}, std::false_type{});
        }
        gu_step(integral_constant<int, 0>{}, std::true_type{}); gu_step(integral_constant<int, 1>{}, std::true_type{});
        gu_step(integral_constant<int, 2>{}, std::true_type{}); gu_step(integral_constant<int, 3>{}, std::true_type{});
        dma_d(2, 4); pinv(pf);
#pragma unroll
        for (int u = 0; u < 7; ++u) acc[u] = MFMA32(wf[1][u], __builtin_bit_cast(bf16x8, xr[3][1]), acc[u]);
    }
    STAMP(3);
    __builtin_amdgcn_s_setprio(PRIO_EP);
    {
        char* dst = ws + WS_AF + ((size_t)rt * NUNIT + 40 + u0) * 1024;
#pragma unroll
        for (int u = 0; u < 7; ++u) {
            float hv[8];
#pragma unroll
            for (int q = 0; q < 8; ++q) {
                const float gt = acc[u][q];
                const float e = __builtin_amdgcn_exp2f(gt * fmaf(gt * gt, -0.1029432397f, -2.302208198f));
                hv[q] = gt * __builtin_amdgcn_rcpf(1.f + e) * acc[u][8 + q];
            }
            gst16c(dst, u * 1024 + l * 16, u32x4{cvtpk(hv[0], hv[1]), cvtpk(hv[2], hv[3]), cvtpk(hv[4], hv[5]), cvtpk(hv[6], hv[7])});
        }
    }
    publish(ws, FGU, rb * 16 + cbk, tag);
    STAMP(4);
    sfor<5>([&](auto Q) { dma_d(3, Q.value); });
    await<16>(ws, FGU, (rb2 >> 1) * 16 + l, tag);
    STAMP(5);
    __builtin_amdgcn_s_setprio(PRIO_DN);
    const char* ab = ws + WS_AF + (size_t)rb2 * 2 * NUNIT * 1024;
    const unsigned aoff = (unsigned)((g >> 1) * 1024 + ((g & 1) * 32 + (l & 15)) * 16);
    u32x4 af[4][4]; u32x2 ae[4][2];
    const unsigned aoff2 = aoff + NUNIT * 1024;
    const char* ap = ab + (40 + 2 * wv) * 1024;
#define ld_a(s, m) do { \
        gld16c<((m) & 1) * 256>(af[(s) & 3][m], ap, (m) >> 1 ? aoff2 : aoff); \
        if ((m) == 3) { \
            if ((s) < 12) ap += 8192; else if ((s) == 12) ap += wv < 2 ? 8192 : 0; \
            else if ((s) == 13) ap = ab + wv * 8192; else ap += 2048; \
            SPIN_(ap); } } while (0)
    sfor<16>([&](auto I) { ld_a(I.value >> 2, I.value & 3); });
    const lds_cptr wb = (lds_cptr)lds + wv * 5120 + l16;
    f32x4 c2[4][5] = {};
    bf16x8 w2[2][5];
#pragma unroll
    for (int n = 0; n < 5; ++n) w2[0][n] = ldsr128(wb + n * 1024);
    const int orow = rb2 * 64 + wv * 16 + (l & 15), ocol = cb2 * 80 + g * 4;
    u32x2 res[5]; f32x4 resf[5];
    __attribute__((address_space(1))) char* op = (__attribute__((address_space(1))) char*)(out + (size_t)orow * DM + ocol);
    const lds_ptr rp = (lds_ptr)lds + wv * 5120 + l * 16;
#define put_c(i) (*(__attribute__((address_space(3))) f32x4*)(rp + (i) / 5 * 20480 + (i) % 5 * 1024) = c2[(i) / 5][(i) % 5])
    const unsigned eo = (unsigned)(32768 + wv * 2048 + ((g >> 1) * 4 + (g & 1)) * 256 + (l & 15) * 8);
    const unsigned fdo = flag_at(FAD, 0, (rb2 >> 2) * 32 + wv * 8 + (l & 7));
#define RETRY_HEAD "v_cmp_eq_u32 vcc, -1, %[t]\n\ts_cbranch_vccz 2f\n\ts_mov_b32 %[n], 0x100000\n" \
    "1:\n\tglobal_load_dwordx2 %[f], %[fo], %[ws] sc1\n\ts_waitcnt vmcnt(0)\n\tv_cmp_ne_u64 vcc, %[tag], %[f]\n\ts_cbranch_vccz 3f\n\t" \
    "s_sleep 2\n\ts_sub_u32 %[n], %[n], 1\n\ts_cbranch_scc0 1b\n3:\n\t"
#define RETRY_TAIL "s_waitcnt vmcnt(0)\n2:"
    sfor<19>([&](auto S_) {
        constexpr int s = S_.value;
        if constexpr (s == 14) {
            LGKM0();
            sfor<5>([&](auto Q) { dma_d(18, Q.value); });
            sfor<5>([&](auto N) { gld8<N.value * 32>(res[N.value], x, (unsigned)(orow * DM + ocol) * 2); });
            wait_vm<0>();
            sfor<5>([&](auto M) { constexpr int n = M.value; pinv(res[n]);
                resf[n] = f32x4{__uint_as_float(res[n][0] << 16), __uint_as_float(res[n][0] & 0xffff0000u), __uint_as_float(res[n][1] << 16), __uint_as_float(res[n][1] & 0xffff0000u)};
                pinv(resf[n]); });
            pinv(op);
            STAMP(6);
            await<32>(ws, FAT, (rb2 >> 2) * 32 + l, tag);
            const unsigned ho = NUNIT * 1024 + 32768 + (wv * 8 + (l & 7)) * 256 + 252;
            unsigned seen = 0;
            sfor<4>([&](auto J) {
                unsigned n, hw;
                asm volatile("s_andn2_b32 %[n], %[need], %[d]\n\ts_cbranch_scc0 9f\n\ts_mov_b32 exec_hi, 0\n\ts_andn2_b32 exec_lo, 0xff, %[d]\n\ts_mov_b32 %[n], 0x80000\n\t"
                             "1:\n\tglobal_load_dword %[h], %[o], %[b] sc1\n\ts_waitcnt vmcnt(0)\n\tv_cmp_ne_u32 vcc, -1, %[h]\n\ts_or_b32 %[d], %[d], vcc_lo\n\t"
                             "s_andn2_b32 vcc_hi, %[need], %[d]\n\ts_cbranch_scc0 8f\n\ts_andn2_b32 exec_lo, 0xff, %[d]\n\ts_sleep %[nap]\n\t"
                             "s_sub_u32 %[n], %[n], 1\n\ts_cbranch_scc0 1b\n8:\n\ts_mov_b64 exec, -1\n9:"
                             : [d] "+s"(seen), [n] "=&s"(n), [h] "=&v"(hw)
                             : [need] "n"(HALL ? 255 : 3 << (2 * J.value)), [o] "v"(ho), [b] "s"(ab), [nap] "n"(HNAP) : "memory", "vcc", "scc");
                sfor<4>([&](auto I) { ld_a(14 + J.value, I.value); });
            });
            STAMP(7);
            sfor<8>([&](auto I) { constexpr int m = I.value >> 1, q = I.value & 1;
                gld8c<q * 512 + (m & 1) * 128>(ae[m][q], ab, eo + (m >> 1) * NUNIT * 1024); });
        }
        constexpr int WAITS[19] = {12, 17, 22, 18, 18, 18, 18, 18, 18, 18, 18, 14, 10, 10, 20, 16, 12, 8, 0};
        SBAR();
        LGKM0(); wait_vm<WAITS[s]>();
        if constexpr (s < 18) sfor<4>([&](auto M) { pinv(af[s & 3][M.value]); });
        else sfor<8>([&](auto M) { pinv(ae[M.value >> 1][M.value & 1]); });
        if constexpr (s >= 14) {
            unsigned t = 0, n; u64 f;
#pragma unroll
            for (int i = 0; i < 16; ++i) t = max(t, s < 18 ? af[s & 3][i >> 2][i & 3] : ae[i >> 2][(i >> 1) & 1][i & 1]);
            if constexpr (s < 18)
                asm volatile(RETRY_HEAD "global_load_dwordx4 %[a0], %[o0], %[b] offset:%[i0] sc1\n\tglobal_load_dwordx4 %[a1], %[o0], %[b] offset:%[i1] sc1\n\t"
                             "global_load_dwordx4 %[a2], %[o1], %[b] offset:%[i0] sc1\n\tglobal_load_dwordx4 %[a3], %[o1], %[b] offset:%[i1] sc1\n\t" RETRY_TAIL
                             : [a0] "+v"(af[s & 3][0]), [a1] "+v"(af[s & 3][1]), [a2] "+v"(af[s & 3][2]), [a3] "+v"(af[s & 3][3]), [n] "=&s"(n), [f] "=&v"(f)
                             : [t] "v"(t), [fo] "v"(fdo), [ws] "s"(ws), [tag] "s"(tag), [o0] "v"(aoff), [o1] "v"(aoff2), [b] "s"(ab + wv * 8192 + 4096),
                               [i0] "n"((s - 16) * 2048), [i1] "n"((s - 16) * 2048 + 256) : "memory", "vcc", "scc");
            else
                asm volatile(RETRY_HEAD "global_load_dwordx2 %[a0], %[o0], %[b] sc1\n\tglobal_load_dwordx2 %[a1], %[o0], %[b] offset:512 sc1\n\t"
                             "global_load_dwordx2 %[a2], %[o0], %[b] offset:128 sc1\n\tglobal_load_dwordx2 %[a3], %[o0], %[b] offset:640 sc1\n\t"
                             "global_load_dwordx2 %[a4], %[o1], %[b] sc1\n\tglobal_load_dwordx2 %[a5], %[o1], %[b] offset:512 sc1\n\t"
                             "global_load_dwordx2 %[a6], %[o1], %[b] offset:128 sc1\n\tglobal_load_dwordx2 %[a7], %[o1], %[b] offset:640 sc1\n\t" RETRY_TAIL
                             : [a0] "+v"(ae[0][0]), [a1] "+v"(ae[0][1]), [a2] "+v"(ae[1][0]), [a3] "+v"(ae[1][1]), [a4] "+v"(ae[2][0]), [a5] "+v"(ae[2][1]),
                               [a6] "+v"(ae[3][0]), [a7] "+v"(ae[3][1]), [n] "=&s"(n), [f] "=&v"(f)
                             : [t] "v"(t), [fo] "v"(fdo), [ws] "s"(ws), [tag] "s"(tag), [o0] "v"(eo), [o1] "v"(eo + NUNIT * 1024), [b] "s"(ab) : "memory", "vcc", "scc");
        }
        if (s == 13 && wv >= 2) {
#pragma unroll
            for (int m = 0; m < 4; ++m) af[1][m] = u32x4{0, 0, 0, 0};
        }
        sfor<4>([&](auto M) {
            constexpr int m = M.value;
            const bf16x8 a = s < 18 ? __builtin_bit_cast(bf16x8, af[s & 3][m])
                                    : __builtin_bit_cast(bf16x8, u32x4{ae[m][0][0], ae[m][0][1], ae[m][1][0], ae[m][1][1]});
            sfor<5>([&](auto N) {
                constexpr int n = N.value;
                SBAR();
                c2[m][n] = MFMA16(w2[s & 1][n], a, c2[m][n]);
                SBAR();
                if (m == 0 && s < 18) w2[(s + 1) & 1][n] = ldsr128(wb + ((s + 1) & 3) * 20480 + n * 1024);
                if constexpr (s < 14 && (((m == 1 || m == 2) && (n & 1)) || (m == 3 && n == 1))) dma_d(s + 4, (m - 1) * 2 + (n >> 1));
                if constexpr (s < 10 && n == 4) ld_a(s + 4, m);
                if constexpr (s == 18 && m * 5 + n >= 4) put_c(m * 5 + n - 4);
            });
        });
        SBAR();
    });
    {
        put_c(16); put_c(17); put_c(18); put_c(19);
        asm volatile("s_waitcnt lgkmcnt(0)\n\ts_barrier" ::: "memory");
        const lds_cptr rq = (lds_cptr)lds + wv * 20480 + l * 16;
        f32x4 pt[5][4];
#pragma unroll
        for (int n = 0; n < 5; ++n)
#pragma unroll
            for (int p = 0; p < 4; ++p) pt[n][p] = *(const __attribute__((address_space(3))) f32x4*)(rq + p * 5120 + n * 1024);
        SBAR();
#pragma unroll
        for (int n = 0; n < 5; ++n) {
            const f32x4 s = (resf[n] + pt[n][0] + pt[n][1]) + (pt[n][2] + pt[n][3]);
            *(__attribute__((address_space(1))) u32x2*)(op + n * 32) = u32x2{cvtpk(s[0], s[1]), cvtpk(s[2], s[3])};
            SBAR();
        }
    }
    STAMP(8);
}

#ifdef __HIP_DEVICE_COMPILE__
#define NO_PK __attribute__((target("no-packed-fp32-ops")))
#else
#define NO_PK
#endif
typedef __attribute__((ext_vector_type(8))) u64 u64x8;
typedef __attribute__((address_space(1))) char* gptr;
#define ARG(i) ((const __hip_bfloat16*)(gptr)ka[i])
#define ARGS() do { asm volatile("s_load_dwordx16 %0, %1, 0x0\n\ts_waitcnt lgkmcnt(0)" : "=&s"(ka) : "s"(kp), "s"(early)); ws = (char*)(gptr)ka[6]; \
    epoch = *(const __attribute__((address_space(4))) u64*)(ka[6] + WS_EPOCH); STAMP(0); } while (0)
__global__ __launch_bounds__(256, 2) NO_PK void fused_block(const __hip_bfloat16* x, const __hip_bfloat16* wqkv, const __hip_bfloat16* wo,
                                                            const __hip_bfloat16* wg, const __hip_bfloat16* wu, const __hip_bfloat16* wd,
                                                            char* ws, __hip_bfloat16* out) {
    __shared__ __attribute__((aligned(16))) char lds[LDS_BYTES];
    u64x8 ka; u64 epoch; unsigned early;
    const auto kp = __builtin_amdgcn_kernarg_segment_ptr();
    const char* code;
    asm volatile("s_load_dword %0, %2, 0x0\n\ts_getpc_b64 %1" : "=&s"(early), "=&s"(code) : "s"(kp));
    const int b = blockIdx.x;
    if (b >= 256) { ARGS(); role_b(code, ARG(0), ARG(1), ARG(2), ARG(3), ARG(4), ARG(5), ws, (__hip_bfloat16*)ARG(7), lds, b - 256, epoch); return; }
    __builtin_amdgcn_s_setprio(PRIO_Q);
    const int xcd = b & 7, i = b >> 3;
    int flag = -1;
    if (i < 30) {
        const int li = (xcd & 1) * 30 + i, rb = (xcd >> 1) * 4 + (li & 3), cb = li >> 2;
        if (cb >= 10) { ARGS(); qkv_block<true>(ARG(0), ARG(1), ws, lds, rb, cb, epoch); }
        else { ARGS(); qkv_block<false>(ARG(0), ARG(1), ws, lds, rb, cb, epoch); }
        flag = cb * 16 + rb;
    } else {
        ARGS();
        if (b < 255) colsum_block(ARG(1), ws, lds, b - 240, epoch);
    }
    asm volatile("" : "+s"(epoch));
    const u64 tag = epoch * 0x9E3779B97F4A7C15ull + 1;
    STAMP(9);
    if (flag >= 0) publish(ws, FQKV, flag, tag);
    if (b == 255) {
        await<256, 1>(ws, FLN, threadIdx.x, tag);
        if (threadIdx.x < 4 * NREP) __hip_atomic_store((gu64*)(ws + flag_at(FGO, threadIdx.x % NREP, threadIdx.x / NREP * 16)), tag, RLX_AGENT);
    }
    if (i >= 30) keep(*(const gu32*)((gptr)code + min(((i - 30) * 256 + (int)threadIdx.x) * 128, CODE_KB * 1024)));
    STAMP(3);
    attn_block(ws, lds, xcd * 4 + (i & 3), i >> 2, tag);
    if (b == 0 && threadIdx.x < 64) {
#pragma nounroll
        for (unsigned spin = 0; spin < SPIN_CAP; ++spin) {
            bool ok = true;
#pragma unroll
            for (int q = 0; q < 4; ++q)
                ok = ok & (__hip_atomic_load((gu64*)(ws + flag_at(FAT, 0, q * 64 + threadIdx.x)), RLX_AGENT) == tag)
                        & (__hip_atomic_load((gu64*)(ws + flag_at(FLN, 0, q * 64 + threadIdx.x)), RLX_AGENT) == tag);
            if (__builtin_amdgcn_ballot_w64(!ok) == 0) break;
            __builtin_amdgcn_s_sleep(2);
        }
        if (threadIdx.x == 0) __hip_atomic_store((gu64*)(ws + WS_EPOCH), tag, RLX_AGENT);
    }
    STAMP(7);
}

extern "C" void kernel_launch(void* const* d_in, const int* in_sizes, int n_in, void* d_out, int out_size, void* d_ws, size_t ws_size,
                              hipStream_t stream) {
    fused_block<<<512, 256, 0, stream>>>((const __hip_bfloat16*)d_in[0], (const __hip_bfloat16*)d_in[1], (const __hip_bfloat16*)d_in[2],
                                         (const __hip_bfloat16*)d_in[4], (const __hip_bfloat16*)d_in[5], (const __hip_bfloat16*)d_in[6],
                                         (char*)d_ws + WSOFF, (__hip_bfloat16*)d_out);
}
```
